```python
import torch

def _softmax(x, axis=-1):
    return torch.softmax(x, dim=axis)
def _gelu(x):
    return torch.nn.functional.gelu(x, approximate="tanh")
def _silu(x):
    return torch.nn.functional.silu(x)
def _sigmoid(x):
    return torch.sigmoid(x)
def _layernorm(x, axis=-1, eps=1e-5):
    m = x.mean(dim=axis, keepdim=True)
    v = x.var(dim=axis, keepdim=True, unbiased=False)
    return (x - m) / torch.sqrt(v + eps)
def _scatter_add(tgt, idx, src, axis=0):
    return tgt.clone().index_add_(axis, idx, src)
def _segment_sum(src, seg, n):
    out = torch.zeros((n,) + src.shape[1:], dtype=src.dtype)
    return out.index_add_(0, seg, src)
def _pad(x, pw):
    flat = tuple(v for pair in reversed(pw) for v in pair)
    return torch.nn.functional.pad(x, flat)

kernel_name = 'recurrent_scan__gqa_block_cached__glu_gated_glu__decode'

def setup_inputs():

    _w = (lambda a: (lambda t: t.to(torch.bfloat16) if t.is_floating_point() else t)(torch.as_tensor(a)))
    def _A_setup():
        B, Sq, D = 8, 1, 1024
        Hq, Hkv, Sk, Dh = 32, 4, 4096, 128
        assert Hq % Hkv == 0, (Hq, Hkv)
        return {
            "x": _w((lambda *s: torch.randn(*s, dtype=torch.bfloat16) * 0.05)(B, Sq, D)),
            "k_cache": _w((lambda *s: torch.randn(*s, dtype=torch.bfloat16) * 1.6)(B, Hkv, Sk, Dh)),
            "v_cache": _w((lambda *s: torch.randn(*s, dtype=torch.bfloat16) * 0.2)(B, Hkv, Sk, Dh)),
            "wq": _w((lambda *s: torch.randn(*s, dtype=torch.bfloat16) * 1.6)(D, Hq * Dh) * (D ** -0.5)),
            "wo": _w((lambda *s: torch.randn(*s, dtype=torch.bfloat16) * 0.2)(Hq * Dh, D) * ((Hq * Dh) ** -0.5)),
        }
    _A_in = _A_setup()

    def _B_setup():
        B, S, D, F = 8, 1, 1024, 3584
        return {
            "x": _w((lambda *s: torch.randn(*s, dtype=torch.bfloat16) * 0.05)(B, S, D)),
            "w_gate": _w((lambda *s: torch.randn(*s, dtype=torch.bfloat16) * 0.2)(D, F)),
            "w_up": _w((lambda *s: torch.randn(*s, dtype=torch.bfloat16) * 0.1)(D, F)),
            "w_down": _w((lambda *s: torch.randn(*s, dtype=torch.bfloat16) * 0.1)(F, D)),
        }
    _B_in = _B_setup()

    return {
        "A_x": _A_in["x"],
        "A_k_cache": _A_in["k_cache"],
        "A_v_cache": _A_in["v_cache"],
        "A_wq": _A_in["wq"],
        "A_wo": _A_in["wo"],
        "B_x": _B_in["x"],
        "B_w_gate": _B_in["w_gate"],
        "B_w_up": _B_in["w_up"],
        "B_w_down": _B_in["w_down"],
    }

def reference(A_x, A_k_cache, A_v_cache, A_wq, A_wo, B_x, B_w_gate, B_w_up, B_w_down):
    def _A_ref(x, k_cache, v_cache, wq, wo):
        B, Sq, D = x.shape
        Hq, Dh = 32, 128
        Hkv = k_cache.shape[1]
        rep = Hq // Hkv
        q = (lambda x, *d: x.permute(*d))((x @ wq).reshape(B, Sq, Hq, Dh), 0, 2, 1, 3)
        k = (lambda x, n, axis: torch.repeat_interleave(x, n, dim=axis))(k_cache, rep, axis=1)
        v = (lambda x, n, axis: torch.repeat_interleave(x, n, dim=axis))(v_cache, rep, axis=1)
        scores = torch.einsum("bhqd,bhkd->bhqk", q, k) * (Dh ** -0.5)
        p = _softmax(scores, axis=-1)
        o = torch.einsum("bhqk,bhkd->bhqd", p, v)
        return (lambda x, *d: x.permute(*d))(o, 0, 2, 1, 3).reshape(B, Sq, Hq * Dh) @ wo

    def _B_ref(x, w_gate, w_up, w_down):
        g = torch.einsum("bsd,df->bsf", x, w_gate)
        u = torch.einsum("bsd,df->bsf", x, w_up)
        h = _sigmoid(g) * u
        return torch.einsum("bsf,fd->bsd", h, w_down)

    x = A_x
    xn = _layernorm(x, axis=-1)
    s = _A_ref(xn, A_k_cache, A_v_cache, A_wq, A_wo)
    g = _silu(_B_ref(xn, B_w_gate, B_w_up, B_w_down))
    return x + s * g
```

```cpp
#include <hip/hip_runtime.h>
#include <hip/hip_bf16.h>
#include <stdint.h>

typedef unsigned short u16;
__device__ __forceinline__ float bf2f(u16 b) { return __uint_as_float(((unsigned)b) << 16); }
__device__ __forceinline__ float rbf(float f) { return __bfloat162float(__float2bfloat16(f)); }

constexpr int NB = 8, D = 1024, HQ = 32, HKV = 4, SK = 4096, DH = 128, F = 3584;

constexpr int NSPLIT = 16;

__global__ void k_ln(const u16* x, float* xn) {
  int b = blockIdx.x; int t = threadIdx.x;
  __shared__ float red[256];
  float v[4]; float s = 0;
  for (int i = 0; i < 4; i++) { v[i] = bf2f(x[b * D + t + 256 * i]); s += v[i]; }
  red[t] = s; __syncthreads();
  for (int o = 128; o > 0; o >>= 1) { if (t < o) red[t] += red[t + o]; __syncthreads(); }
  float mean = red[0] / D; __syncthreads();
  float s2 = 0;
  for (int i = 0; i < 4; i++) { float d = v[i] - mean; s2 += d * d; }
  red[t] = s2; __syncthreads();
  for (int o = 128; o > 0; o >>= 1) { if (t < o) red[t] += red[t + o]; __syncthreads(); }
  float var = red[0] / D;
  float mb = rbf(mean), vb = rbf(var);
  float den = rbf(sqrtf(rbf(vb + 1e-5f)));
  for (int i = 0; i < 4; i++) xn[b * D + t + 256 * i] = rbf(rbf(v[i] - mb) / den);
}

__global__ void k_gemv(const float* a, const u16* w, float* out, int K, int N) {
  int n = blockIdx.x * 64 + (threadIdx.x & 63);
  int kq = threadIdx.x >> 6;
  float acc[8] = {0};
  int k0 = kq * (K / 4), k1 = k0 + K / 4;
  for (int k = k0; k < k1; k++) {
    float wv = bf2f(w[(size_t)k * N + n]);
    for (int b = 0; b < 8; b++) acc[b] += a[b * K + k] * wv;
  }
  __shared__ float red[4][8][64];
  for (int b = 0; b < 8; b++) red[kq][b][threadIdx.x & 63] = acc[b];
  __syncthreads();
  if (kq == 0) for (int b = 0; b < 8; b++) {
    int l = threadIdx.x;
    out[b * N + n] = rbf(red[0][b][l] + red[1][b][l] + red[2][b][l] + red[3][b][l]);
  }
}

__global__ void k_h(const float* g, const float* u, float* h) {
  int i = blockIdx.x * 256 + threadIdx.x;
  float sg = rbf(1.f / (1.f + __expf(-g[i])));
  h[i] = rbf(sg * u[i]);
}

__global__ void k_attn(const float* q, const u16* kc, const u16* vc, float* po, float* ml) {
  int s = blockIdx.x, hq = blockIdx.y, b = blockIdx.z;
  int hkv = hq / 8;
  int t = threadIdx.x;
  __shared__ float qs[128]; __shared__ float p[256]; __shared__ float red[256];
  if (t < 128) qs[t] = q[b * 4096 + hq * 128 + t];
  __syncthreads();
  const u16* kp = kc + ((size_t)(b * HKV + hkv) * SK + s * 256 + t) * DH;
  float acc = 0;
  for (int d = 0; d < 128; d++) acc += qs[d] * bf2f(kp[d]);
  float sc = rbf(rbf(acc) * 0.08838834764831845f);
  red[t] = sc; __syncthreads();
  for (int o = 128; o > 0; o >>= 1) { if (t < o) red[t] = fmaxf(red[t], red[t + o]); __syncthreads(); }
  float m = red[0]; __syncthreads();
  float e = __expf(sc - m);
  p[t] = e; red[t] = e; __syncthreads();
  for (int o = 128; o > 0; o >>= 1) { if (t < o) red[t] += red[t + o]; __syncthreads(); }
  float l = red[0];
  if (t < 128) {
    const u16* vp = vc + ((size_t)(b * HKV + hkv) * SK + s * 256) * DH + t;
    float o = 0;
    for (int k = 0; k < 256; k++) o += p[k] * bf2f(vp[(size_t)k * DH]);
    po[((size_t)(b * HQ + hq) * NSPLIT + s) * 128 + t] = o;
  }
  if (t == 0) { ml[((b * HQ + hq) * NSPLIT + s) * 2] = m; ml[((b * HQ + hq) * NSPLIT + s) * 2 + 1] = l; }
}

__global__ void k_comb(const float* po, const float* ml, float* o) {
  int bh = blockIdx.x; int t = threadIdx.x;
  float M = -INFINITY;
  for (int s = 0; s < NSPLIT; s++) M = fmaxf(M, ml[(bh * NSPLIT + s) * 2]);
  float L = 0, acc = 0;
  for (int s = 0; s < NSPLIT; s++) {
    float w = __expf(ml[(bh * NSPLIT + s) * 2] - M);
    L += w * ml[(bh * NSPLIT + s) * 2 + 1];
    acc += w * po[((size_t)bh * NSPLIT + s) * 128 + t];
  }
  o[bh * 128 + t] = rbf(acc / L);
}

__global__ void k_final(const u16* x, const float* s, const float* d, __hip_bfloat16* out) {
  int i = blockIdx.x * 256 + threadIdx.x;
  float dv = d[i];
  float g = rbf(dv / (1.f + __expf(-dv)));
  float sg = rbf(s[i] * g);
  out[i] = __float2bfloat16(bf2f(x[i]) + sg);
}

extern "C" void kernel_launch(void* const* d_in, const int* in_sizes, int n_in,
                              void* d_out, int out_size, void* d_ws, size_t ws_size,
                              hipStream_t stream) {
  const u16* x = (const u16*)d_in[0];
  const u16* kc = (const u16*)d_in[1];
  const u16* vc = (const u16*)d_in[2];
  const u16* wq = (const u16*)d_in[3];
  const u16* wo = (const u16*)d_in[4];
  const u16* wg = (const u16*)d_in[6];
  const u16* wu = (const u16*)d_in[7];
  const u16* wd = (const u16*)d_in[8];
  float* ws = (float*)d_ws;
  float* xn = ws; float* q = xn + 8 * 1024; float* g = q + 8 * 4096; float* u = g + 8 * F; float* h = u + 8 * F;
  float* po = h + 8 * F; float* ml = po + 8 * 32 * NSPLIT * 128; float* o = ml + 8 * 32 * NSPLIT * 2;
  float* s = o + 8 * 4096; float* dd = s + 8 * 1024;
  k_ln<<<8, 256, 0, stream>>>(x, xn);
  k_gemv<<<4096 / 64, 256, 0, stream>>>(xn, wq, q, 1024, 4096);
  k_gemv<<<F / 64, 256, 0, stream>>>(xn, wg, g, 1024, F);
  k_gemv<<<F / 64, 256, 0, stream>>>(xn, wu, u, 1024, F);
  k_h<<<8 * F / 256, 256, 0, stream>>>(g, u, h);
  k_attn<<<dim3(NSPLIT, HQ, NB), 256, 0, stream>>>(q, kc, vc, po, ml);
  k_comb<<<256, 128, 0, stream>>>(po, ml, o);
  k_gemv<<<1024 / 64, 256, 0, stream>>>(o, wo, s, 4096, 1024);
  k_gemv<<<1024 / 64, 256, 0, stream>>>(h, wd, dd, F, 1024);
  k_final<<<8192 / 256, 256, 0, stream>>>(x, s, dd, (__hip_bfloat16*)d_out);
}
```

```cpp
#include <hip/hip_runtime.h>
#include <stdint.h>

typedef unsigned int u32;
typedef unsigned long long u64;
typedef unsigned short u16;
typedef __attribute__((ext_vector_type(4))) unsigned int u32x4;
typedef __attribute__((ext_vector_type(2))) unsigned int u32x2;
typedef __attribute__((ext_vector_type(4))) float f32x4;
typedef __attribute__((ext_vector_type(8))) __bf16 bf16x8;
typedef __attribute__((ext_vector_type(2))) __bf16 bf16x2;
typedef __amdgpu_buffer_rsrc_t rsrc_t;

#define STAMPS 0

constexpr int DM = 1024, NQ = 4096, FF = 3584;
constexpr u32 KV_BYTES = 8u * 4u * 4096u * 128u * 2u;
constexpr u32 WQ_BYTES = 1024u * 4096u * 2u;
constexpr u32 WG_BYTES = 1024u * 3584u * 2u;

constexpr u32 WS_EPOCH = 0;
constexpr u32 WS_FLAGS = 4096;
constexpr u32 WS_QP = 65536;
constexpr u32 WS_GP = WS_QP + 4u * 8u * 4096u * 4u;
constexpr u32 WS_OP = WS_GP + 4u * 8u * 3584u * 4u;
constexpr u32 WS_ST = WS_OP + 32u * 8u * 8u * 256u;
constexpr u32 WS_FP = WS_ST + 32u * 8u * 8u * 8u;
constexpr u32 WS_END = WS_FP + 256u * 32u * 128u;

constexpr int AUX_NT = 2, AUX_SC1 = 16;
constexpr u32 SENT = 0xFFFFFFFFu;
constexpr int LDS_SPIN_CAP = 1 << 21;
constexpr int GLB_SPIN_CAP = 1 << 17;

enum { C_P1 = 0, C_FLAGS, C_Q, C_KVI, C_KVD, C_D3, C_O3, C_KVA, C_N };

#define CBAR() asm volatile("" ::: "memory")
#define WIN 7
#define WIN1 WIN
#define WIN3 WIN
template <int N> __device__ __forceinline__ void wait_vm() { asm volatile("s_waitcnt vmcnt(%0)" ::"n"(N) : "memory"); }

__device__ __forceinline__ rsrc_t mk_rsrc(const void* p, u32 bytes) {
  return __builtin_amdgcn_make_buffer_rsrc((void*)p, (short)0, (int)bytes, 0x00020000);
}
__device__ __forceinline__ u32x4 ld_nt(rsrc_t r, u32 voff, u32 soff) { return __builtin_amdgcn_raw_buffer_load_b128(r, (int)voff, (int)soff, AUX_NT); }
__device__ __forceinline__ u32x4 ld_sc1(rsrc_t r, u32 voff, u32 soff) { return __builtin_amdgcn_raw_buffer_load_b128(r, (int)voff, (int)soff, AUX_SC1); }
__device__ __forceinline__ void st_sc1(rsrc_t r, u32 voff, u32x4 v) { __builtin_amdgcn_raw_buffer_store_b128(v, r, (int)voff, 0, AUX_SC1); }
__device__ __forceinline__ void st_sc1_64(rsrc_t r, u32 voff, u32x2 v) { __builtin_amdgcn_raw_buffer_store_b64(v, r, (int)voff, 0, AUX_SC1); }

__device__ __forceinline__ float u2f(u32 x) { return __uint_as_float(x); }
__device__ __forceinline__ u32 f2u(float x) { return __float_as_uint(x); }
__device__ __forceinline__ float bflo(u32 x) { return u2f(x << 16); }
__device__ __forceinline__ float bfhi(u32 x) { return u2f(x & 0xFFFF0000u); }
__device__ __forceinline__ u32 pack2(float lo, float hi) {
  bf16x2 v = {(__bf16)lo, (__bf16)hi};
  return __builtin_bit_cast(u32, v);
}
__device__ __forceinline__ float rbf(float x) { return (float)(__bf16)x; }
template <int CTRL> __device__ __forceinline__ float dpp(float x) {
  return __builtin_bit_cast(float, __builtin_amdgcn_mov_dpp(__builtin_bit_cast(int, x), CTRL, 0xf, 0xf, true));
}
constexpr int XOR1 = 0xB1, XOR2 = 0x4E, XOR7 = 0x141, XOR8 = 0x128;
__device__ __forceinline__ float xrow_max(float x) {
  auto s = __builtin_amdgcn_permlane16_swap(f2u(x), f2u(x), false, false);
  x = fmaxf(u2f(s[0]), u2f(s[1]));
  auto t = __builtin_amdgcn_permlane32_swap(f2u(x), f2u(x), false, false);
  return fmaxf(u2f(t[0]), u2f(t[1]));
}
__device__ __forceinline__ float xrow_sum(float x) {
  auto s = __builtin_amdgcn_permlane16_swap(f2u(x), f2u(x), false, false);
  x = u2f(s[0]) + u2f(s[1]);
  auto t = __builtin_amdgcn_permlane32_swap(f2u(x), f2u(x), false, false);
  return u2f(t[0]) + u2f(t[1]);
}
__device__ __forceinline__ float wave_sum(float x) {
  x += dpp<XOR1>(x); x += dpp<XOR2>(x); x += dpp<XOR7>(x); x += dpp<XOR8>(x);
  return xrow_sum(x);
}
__device__ __forceinline__ f32x4 mfma16(u32x4 a, u32x4 b, f32x4 c) {
  return __builtin_amdgcn_mfma_f32_16x16x32_bf16(__builtin_bit_cast(bf16x8, a), __builtin_bit_cast(bf16x8, b), c, 0, 0, 0);
}
__device__ __forceinline__ u32x4 tcol(const u32x4* R, int t) {
  const u32 sel = (t & 1) ? 0x07060302u : 0x05040100u;
  const int d = t >> 1;
  u32x4 r;
  r[0] = __builtin_amdgcn_perm(R[1][d], R[0][d], sel);
  r[1] = __builtin_amdgcn_perm(R[3][d], R[2][d], sel);
  r[2] = __builtin_amdgcn_perm(R[5][d], R[4][d], sel);
  r[3] = __builtin_amdgcn_perm(R[7][d], R[6][d], sel);
  return r;
}
__device__ __forceinline__ u32 umax3(u32 a, u32 b, u32 c) { return max(max(a, b), c); }
__device__ __forceinline__ u32 umax4(u32x4 v) { return max(umax3(v[0], v[1], v[2]), v[3]); }
__device__ __forceinline__ u32x4 canon(u32x4 v) {
  v[0] = min(v[0], 0xFFFFFFFEu); v[1] = min(v[1], 0xFFFFFFFEu); v[2] = min(v[2], 0xFFFFFFFEu); v[3] = min(v[3], 0xFFFFFFFEu);
  return v;
}

__device__ __forceinline__ void lds_wait(u32* p, u32 target) {
  CBAR();
  for (int i = 0; i < LDS_SPIN_CAP; i++) {
    u32 v = __hip_atomic_load(p, __ATOMIC_RELAXED, __HIP_MEMORY_SCOPE_WORKGROUP);
    if (__builtin_amdgcn_readfirstlane(v) >= target) break;
    __builtin_amdgcn_s_sleep(1);
  }
  CBAR();
}
__device__ __forceinline__ u32 lds_arrive(u32* p, int lane) {
  asm volatile("s_waitcnt lgkmcnt(0)" ::: "memory");
  u32 old = 0;
  if (lane == 0) old = __hip_atomic_fetch_add(p, 1u, __ATOMIC_RELAXED, __HIP_MEMORY_SCOPE_WORKGROUP);
  old = __builtin_amdgcn_readfirstlane(old);
  CBAR();
  return old;
}

constexpr int XN_STRIDE = 1024 + 8;
constexpr int SL_STRIDE = 68;
constexpr int Q_STRIDE = 136;

struct __align__(16) Lds {
  u16 xn[8 * XN_STRIDE];
  float slab1[2 * 4 * 8 * SL_STRIDE];
  float slab3[2 * 4 * 8 * SL_STRIDE];
  float kvs[8 * 8 * 128];
  float kvst[8 * 8 * 2];
  u16 qs[8 * Q_STRIDE];
  u32 ctrl[16];
  u64 tag[2];
};

__device__ __forceinline__ void w_epilogue(const f32x4* acc, float* slab, int lane) {
  float val[2][8];
#pragma unroll
  for (int t = 0; t < 8; t++) {
#pragma unroll
    for (int r = 0; r < 2; r++) {
      auto s = __builtin_amdgcn_permlane32_swap(f2u(acc[t][r]), f2u(acc[t][r + 2]), false, false);
      val[r][t] = u2f(s[0]) + dpp<XOR8>(u2f(s[1]));
    }
  }
  if (!(lane & 8)) {
    const int b0 = 4 * ((lane >> 4) & 1) + 2 * (lane >> 5);
    const int cp = lane & 7;
#pragma unroll
    for (int r = 0; r < 2; r++) {
      float* p = slab + (b0 + r) * SL_STRIDE + 8 * cp;
      *(f32x4*)p = f32x4{val[r][0], val[r][1], val[r][2], val[r][3]};
      *(f32x4*)(p + 4) = f32x4{val[r][4], val[r][5], val[r][6], val[r][7]};
    }
  }
}
__device__ __forceinline__ f32x4 slab_sum(const float* slab, int b, int col) {
  const float* p = slab + b * SL_STRIDE + col;
  f32x4 a = *(const f32x4*)p;
  f32x4 bb = *(const f32x4*)(p + 8 * SL_STRIDE);
  f32x4 c = *(const f32x4*)(p + 16 * SL_STRIDE);
  f32x4 d = *(const f32x4*)(p + 24 * SL_STRIDE);
  return (a + bb) + (c + d);
}

#define STAMP(i) do { } while (0)

__global__ void __launch_bounds__(768, 3)
fused_decode_kernel(const u16* x, const u16* kc, const u16* vc,
                    const u16* wq, const u16* wo, const u16* wg,
                    const u16* wu, const u16* wd, u16* out,
                    unsigned char* ws) {
  __shared__ Lds L;
  const int tid = threadIdx.x;
  const int lane = tid & 63;
  const int wave = __builtin_amdgcn_readfirstlane(tid >> 6);
  const int c = blockIdx.x;
  const int xc = c & 7, j = c >> 3;
  const int ab = xc, ahkv = j >> 3, as = j & 7;
  const int kb_o = xc * 2 + (j >> 4), ct_o = j & 15;
  const bool mlp = c < 224;
  const int ud = xc * 28 + j;
  const int kb_d = ud >> 4, ct_d = ud & 15;
  const rsrc_t rws = mk_rsrc(ws, WS_END);

  if (wave >= 4) {
    const int v = wave - 4;
    __builtin_amdgcn_s_setprio(3);
    if (v == 0) STAMP(0);
    const rsrc_t rx = mk_rsrc(x, 8u * 1024u * 2u);
    u32x4 xa = __builtin_amdgcn_raw_buffer_load_b128(rx, v * 2048 + lane * 16, 0, 0);
    u32x4 xb = __builtin_amdgcn_raw_buffer_load_b128(rx, v * 2048 + 1024 + lane * 16, 0, 0);
    const u32x2 ep = __builtin_amdgcn_raw_buffer_load_b64(rws, v == 0 ? WS_EPOCH : 0xFFFFFFF0u, 0, AUX_SC1);
    const int hq = ahkv * 8 + v;
    const u32 op_off = WS_OP + (u32)((hq * 8 + ab) * 8 + as) * 256u;
    const u32 st_off = WS_ST + (u32)((hq * 8 + ab) * 8 + as) * 8u;
    {
      u32 off = 0xFFFFFFF0u;
      if (lane < 16) off = op_off + lane * 16;
      else if (lane < 48) {
        const int chunk = 4 * v + ((lane - 16) >> 3), piece = lane & 7;
        if (chunk < 16) off = WS_FP + (u32)((ct_o * 16 + chunk) * 32 + kb_o) * 128u + piece * 16;
        else if (mlp) off = WS_FP + (u32)((ct_d * 16 + chunk - 16) * 32 + 16 + kb_d) * 128u + piece * 16;
      }
      if (off != 0xFFFFFFF0u) st_sc1(rws, off, u32x4{SENT, SENT, SENT, SENT});
      if (lane == 48) st_sc1_64(rws, st_off, u32x2{SENT, SENT});
    }
    if (v == 1 && lane < 16) L.ctrl[lane] = 0;
    {
      float e[16];
#pragma unroll
      for (int i = 0; i < 4; i++) { e[2 * i] = bflo(xa[i]); e[2 * i + 1] = bfhi(xa[i]); e[8 + 2 * i] = bflo(xb[i]); e[9 + 2 * i] = bfhi(xb[i]); }
      float s = 0.f;
#pragma unroll
      for (int i = 0; i < 16; i++) s += e[i];
      const float mean = wave_sum(s) * (1.f / 1024.f);
      float s2 = 0.f;
#pragma unroll
      for (int i = 0; i < 16; i++) { float d = e[i] - mean; s2 += d * d; }
      const float var = wave_sum(s2) * (1.f / 1024.f);
      const float mb = rbf(mean), vb = rbf(var);
      const float den = rbf(sqrtf(rbf(vb + 1e-5f)));
      u32x4 ya, yb;
#pragma unroll
      for (int i = 0; i < 4; i++) {
        ya[i] = pack2(rbf(e[2 * i] - mb) / den, rbf(e[2 * i + 1] - mb) / den);
        yb[i] = pack2(rbf(e[8 + 2 * i] - mb) / den, rbf(e[9 + 2 * i] - mb) / den);
      }
      *(u32x4*)&L.xn[v * XN_STRIDE + lane * 8] = ya;
      *(u32x4*)&L.xn[v * XN_STRIDE + 512 + lane * 8] = yb;
    }
    if (v == 0 && lane == 0) {
      u64 E = ((u64)ep[1] << 32) | ep[0];
      L.tag[0] = E * 6364136223846793005ull + 1442695040888963407ull;
    }
    asm volatile("s_waitcnt vmcnt(0) lgkmcnt(0)" ::: "memory");
    __builtin_amdgcn_s_barrier();
    CBAR();
    if (v == 0) STAMP(1);
    __builtin_amdgcn_s_setprio(0);
    const rsrc_t rk = mk_rsrc(kc, KV_BYTES), rv = mk_rsrc(vc, KV_BYTES);
    const u32 kvbase = (u32)((ab * 4 + ahkv) * 4096 + as * 512 + v * 64) * 256u;
    const int rho = lane & 15, g = lane >> 4;
    const u32 koff = (u32)(4 * (rho & 3) + (rho >> 2)) * 256u + g * 16;
    u32x4 K[4][4], V[2][8];
    if (v >= 4) lds_wait(&L.ctrl[C_KVI], 4);
#pragma unroll
    for (int T = 0; T < 4; T++)
#pragma unroll
      for (int st = 0; st < 4; st++) {
        if (T * 4 + st >= WIN) wait_vm<WIN - 1>();
        K[T][st] = ld_nt(rk, koff + st * 64, kvbase + T * 4096);
      }
#pragma unroll
    for (int u = 0; u < 2; u++)
#pragma unroll
      for (int i = 0; i < 8; i++) {
        wait_vm<WIN - 1>();
        V[u][i] = ld_nt(rv, lane * 16 + (i & 3) * 1024, kvbase + u * 8192 + (i >> 2) * 4096);
      }
    __builtin_amdgcn_sched_barrier(0);
    __builtin_amdgcn_s_setprio(2);
    lds_arrive(&L.ctrl[C_KVI], lane);
    if (v == 7) STAMP(2);
    lds_wait(&L.ctrl[C_Q], 4);
    if (v == 0) STAMP(3);
    f32x4 sa[4];
#pragma unroll
    for (int T = 0; T < 4; T++) sa[T] = f32x4{0.f, 0.f, 0.f, 0.f};
#pragma unroll
    for (int st = 0; st < 4; st++) {
      const u32x4 qf = *(const u32x4*)&L.qs[(lane & 7) * Q_STRIDE + st * 32 + g * 8];
#pragma unroll
      for (int T = 0; T < 4; T++) sa[T] = mfma16(K[T][st], qf, sa[T]);
    }
    float sc[16];
    float m = -INFINITY;
#pragma unroll
    for (int T = 0; T < 4; T++)
#pragma unroll
      for (int r = 0; r < 4; r++) { float z = rbf(rbf(sa[T][r]) * 0.08838834764831845f); sc[T * 4 + r] = z; m = fmaxf(m, z); }
    m = xrow_max(m);
    float lsum = 0.f;
#pragma unroll
    for (int i = 0; i < 16; i++) { sc[i] = __expf(sc[i] - m); lsum += sc[i]; }
    lsum = xrow_sum(lsum);
    f32x4 oa[8];
#pragma unroll
    for (int t = 0; t < 8; t++) oa[t] = f32x4{0.f, 0.f, 0.f, 0.f};
#pragma unroll
    for (int u = 0; u < 2; u++) {
      u32x4 pf;
#pragma unroll
      for (int i = 0; i < 4; i++) pf[i] = pack2(sc[8 * u + 2 * i], sc[8 * u + 2 * i + 1]);
#pragma unroll
      for (int t = 0; t < 8; t++) oa[t] = mfma16(pf, tcol(V[u], t), oa[t]);
    }
    if (g < 2) {
#pragma unroll
      for (int r = 0; r < 4; r++) {
        float* p = &L.kvs[((v * 8 + 4 * g + r) * 2) * 64 + rho * 4];
        *(f32x4*)p = f32x4{oa[0][r], oa[1][r], oa[2][r], oa[3][r]};
        *(f32x4*)(p + 64) = f32x4{oa[4][r], oa[5][r], oa[6][r], oa[7][r]};
      }
    }
    if (lane < 8) { L.kvst[(lane * 8 + v) * 2] = m; L.kvst[(lane * 8 + v) * 2 + 1] = lsum; }
    lds_arrive(&L.ctrl[C_KVD], lane);
    lds_wait(&L.ctrl[C_KVD], 8);
    if (v == 0) STAMP(4);
    {
      const f32x4* sp = (const f32x4*)&L.kvst[v * 16];
      const f32x4 s0 = sp[0], s1 = sp[1], s2 = sp[2], s3 = sp[3];
      const f32x4 sm = sp[g];
      const float M = fmaxf(fmaxf(fmaxf(s0[0], s0[2]), fmaxf(s1[0], s1[2])), fmaxf(fmaxf(s2[0], s2[2]), fmaxf(s3[0], s3[2])));
      const float e0 = __expf(sm[0] - M), e1 = __expf(sm[2] - M);
      const float Ls = xrow_sum(e0 * sm[1] + e1 * sm[3]);
      const float* p0 = &L.kvs[(((2 * g) * 8 + v) * 2) * 64 + rho * 4];
      const float* p1 = p0 + 8 * 128;
      f32x4 a0 = *(const f32x4*)p0, a1 = *(const f32x4*)(p0 + 64), b0 = *(const f32x4*)p1, b1 = *(const f32x4*)(p1 + 64);
      const float inv = 1.f / Ls;
      float o[8];
#pragma unroll
      for (int i = 0; i < 4; i++) {
        o[i] = xrow_sum(e0 * a0[i] + e1 * b0[i]) * inv;
        o[4 + i] = xrow_sum(e0 * a1[i] + e1 * b1[i]) * inv;
      }
      u32x4 ov = {pack2(o[0], o[1]), pack2(o[2], o[3]), pack2(o[4], o[5]), pack2(o[6], o[7])};
      ov = canon(ov);
      if (lane < 16) st_sc1(rws, op_off + lane * 16, ov);
      if (lane == 16) st_sc1_64(rws, st_off, u32x2{min(f2u(M), 0xFFFFFFFEu), min(f2u(Ls), 0xFFFFFFFEu)});
    }
    if (v == 0) STAMP(5);
    return;
  }

  const int w = wave;
  const int cp = lane & 7, hi = (lane >> 3) & 1, g = lane >> 4;
  const int ct_q = c >> 2, ks_q = c & 3;
  const int mat = c >= 112 ? 1 : 0;
  const int mm = c - 112 * mat;
  const int ct_g = mm >> 1, ks_g = mm & 1;
  u32x2 xfin;
  {
    const rsrc_t rx = mk_rsrc(x, 8u * 1024u * 2u);
    xfin = __builtin_amdgcn_raw_buffer_load_b64(rx, (lane & 7) * 2048 + c * 8, 0, 0);
  }
  {
    u32x4 Rq[8], Rg[2][8];
    const rsrc_t rq = mk_rsrc(wq, WQ_BYTES);
    const u32 qoff = (u32)(16 * g + 8 * hi) * 8192u + cp * 16;
    const u32 qbase = (u32)(ks_q * 256 + w * 64) * 8192u + ct_q * 128;
#pragma unroll
    for (int i = 0; i < 8; i++) { if (i >= WIN1) wait_vm<WIN1 - 1>(); Rq[i] = ld_nt(rq, qoff, qbase + i * 8192); }
    {
      const rsrc_t rg = mk_rsrc(mat ? wu : wg, mlp ? WG_BYTES : 0u);
      const u32 goff = (u32)(16 * g + 8 * hi) * 7168u + cp * 16;
      const u32 gbase = (u32)(ks_g * 512 + w * 128) * 7168u + ct_g * 128;
#pragma unroll
      for (int tl = 0; tl < 2; tl++)
#pragma unroll
        for (int i = 0; i < 8; i++) { if (8 + tl * 8 + i >= WIN1) wait_vm<WIN1 - 1>(); Rg[tl][i] = ld_nt(rg, goff, gbase + (tl * 64 + i) * 7168); }
    }
    __builtin_amdgcn_sched_barrier(0);
    CBAR();
    __builtin_amdgcn_s_setprio(3);
    __builtin_amdgcn_s_barrier();
    CBAR();
    if (w == 0) STAMP(8);
    const int r = lane & 15;
    const u16* xrow = &L.xn[(r & 7) * XN_STRIDE + 16 * g + 8 * (r >> 3)];
    {
      const u32x4 af = *(const u32x4*)(xrow + ks_q * 256 + w * 64);
      f32x4 acc[8];
#pragma unroll
      for (int t = 0; t < 8; t++) acc[t] = mfma16(af, tcol(Rq, t), f32x4{0.f, 0.f, 0.f, 0.f});
      w_epilogue(acc, &L.slab1[(0 * 4 + w) * 8 * SL_STRIDE], lane);
    }
    {
      const u32x4 a0 = *(const u32x4*)(xrow + ks_g * 512 + w * 128);
      const u32x4 a1 = *(const u32x4*)(xrow + ks_g * 512 + w * 128 + 64);
      f32x4 acc[8];
#pragma unroll
      for (int t = 0; t < 8; t++) acc[t] = mfma16(a0, tcol(Rg[0], t), f32x4{0.f, 0.f, 0.f, 0.f});
#pragma unroll
      for (int t = 0; t < 8; t++) acc[t] = mfma16(a1, tcol(Rg[1], t), acc[t]);
      w_epilogue(acc, &L.slab1[(1 * 4 + w) * 8 * SL_STRIDE], lane);
    }
  }
  if (lds_arrive(&L.ctrl[C_P1], lane) == 3) {
    STAMP(9);
#pragma unroll
    for (int i = 0; i < 2; i++) {
      const int b = g + 4 * i, col = 4 * (lane & 15);
      f32x4 s = slab_sum(&L.slab1[0], b, col);
      st_sc1(rws, WS_QP + (u32)((ks_q * 8 + b) * 4096 + ct_q * 64 + col) * 4u, __builtin_bit_cast(u32x4, s));
      if (mlp) {
        f32x4 s2 = slab_sum(&L.slab1[4 * 8 * SL_STRIDE], b, col);
        st_sc1(rws, WS_GP + (u32)(((mat * 2 + ks_g) * 8 + b) * 3584 + ct_g * 64 + col) * 4u, __builtin_bit_cast(u32x4, s2));
      }
    }
    const u64 tag = L.tag[0];
    const u32 tlo = (u32)tag, thi = (u32)(tag >> 32);
    STAMP(6);
    asm volatile("s_waitcnt vmcnt(0)" ::: "memory");
    STAMP(7);
    if (lane == 0) st_sc1_64(rws, WS_FLAGS + c * 8, u32x2{tlo, thi});
    STAMP(10);
    for (int it = 0; it < GLB_SPIN_CAP; it++) {
      CBAR();
      u32x4 f0 = ld_sc1(rws, WS_FLAGS + lane * 32, 0);
      u32x4 f1 = ld_sc1(rws, WS_FLAGS + lane * 32 + 16, 0);
      const u32 bad = ((f0[0] ^ tlo) | (f0[1] ^ thi) | (f0[2] ^ tlo) | (f0[3] ^ thi)) |
                      ((f1[0] ^ tlo) | (f1[1] ^ thi) | (f1[2] ^ tlo) | (f1[3] ^ thi));
      if (__all(bad == 0)) break;
#define PSLEEP 8
      __builtin_amdgcn_s_sleep(PSLEEP);
    }
    CBAR();
    if (lane == 0) __hip_atomic_store(&L.ctrl[C_FLAGS], 1u, __ATOMIC_RELAXED, __HIP_MEMORY_SCOPE_WORKGROUP);
    if (c == 0 && lane == 0) st_sc1_64(rws, WS_EPOCH, u32x2{tlo, thi});
    STAMP(11);
  } else {
    lds_wait(&L.ctrl[C_FLAGS], 1);
  }
  u32x4 hfrag = {0u, 0u, 0u, 0u};
  {
    const u32 qo = WS_QP + (u32)(ab * 4096 + ahkv * 1024 + w * 256 + 4 * lane) * 4u;
    const rsrc_t rwq = rws;
    u32x4 q0 = ld_sc1(rwq, qo, 0), q1 = ld_sc1(rwq, qo, 131072), q2 = ld_sc1(rwq, qo, 262144), q3 = ld_sc1(rwq, qo, 393216);
    u32x4 gp[4][2];
    {
      const rsrc_t rwh = mk_rsrc(ws, mlp ? WS_END : 0u);
      const u32 ho = WS_GP + (u32)((lane & 7) * 3584 + kb_d * 256 + w * 64 + 4 * (2 * g + hi)) * 4u;
#pragma unroll
      for (int p = 0; p < 4; p++) {
        gp[p][0] = ld_sc1(rwh, ho, p * 114688);
        gp[p][1] = ld_sc1(rwh, ho + 128, p * 114688);
      }
    }
    __builtin_amdgcn_sched_barrier(0);
    f32x4 qs = (__builtin_bit_cast(f32x4, q0) + __builtin_bit_cast(f32x4, q1)) + (__builtin_bit_cast(f32x4, q2) + __builtin_bit_cast(f32x4, q3));
    *(u32x2*)&L.qs[(2 * w + (lane >> 5)) * Q_STRIDE + ((4 * lane) & 127)] = u32x2{pack2(qs[0], qs[1]), pack2(qs[2], qs[3])};
    lds_arrive(&L.ctrl[C_Q], lane);
    if (w == 0) STAMP(12);
    {
      float hv[8];
#pragma unroll
      for (int hf = 0; hf < 2; hf++) {
        f32x4 G = __builtin_bit_cast(f32x4, gp[0][hf]) + __builtin_bit_cast(f32x4, gp[1][hf]);
        f32x4 U = __builtin_bit_cast(f32x4, gp[2][hf]) + __builtin_bit_cast(f32x4, gp[3][hf]);
#pragma unroll
        for (int i = 0; i < 4; i++) {
          const float gg = rbf(G[i]), uu = rbf(U[i]);
          const float sg = rbf(1.f / (1.f + __expf(-gg)));
          hv[hf * 4 + i] = sg * uu;
        }
      }
      hfrag = u32x4{pack2(hv[0], hv[1]), pack2(hv[2], hv[3]), pack2(hv[4], hv[5]), pack2(hv[6], hv[7])};
    }
  }
  lds_wait(&L.ctrl[C_KVI], 8);
  if (w == 0) STAMP(13);
  __builtin_amdgcn_s_setprio(0);
  u32x4 Rd[8], Ro[8];
  {
    const rsrc_t rd = mk_rsrc(wd, mlp ? WG_BYTES : 0u);
    const u32 doff = (u32)(4 * (2 * g + hi)) * 2048u + cp * 16;
    const u32 dbase = (u32)(kb_d * 256 + w * 64) * 2048u + ct_d * 128;
#pragma unroll
    for (int i = 0; i < 8; i++) { if (i >= WIN3) wait_vm<WIN3 - 1>(); Rd[i] = ld_nt(rd, doff, dbase + (32 * (i >> 2) + (i & 3)) * 2048); }
  }
  {
    const rsrc_t ro = mk_rsrc(wo, WQ_BYTES);
    const u32 ooff = (u32)(16 * g + 8 * hi) * 2048u + cp * 16;
    const u32 obase = (u32)(kb_o * 256 + w * 64) * 2048u + ct_o * 128;
#pragma unroll
    for (int i = 0; i < 8; i++) { if (8 + i >= WIN3) wait_vm<WIN3 - 1>(); Ro[i] = ld_nt(ro, ooff, obase + i * 2048); }
  }
  __builtin_amdgcn_sched_barrier(0);
  __builtin_amdgcn_s_setprio(1);
  if (mlp) {
    f32x4 acc[8];
#pragma unroll
    for (int t = 0; t < 8; t++) acc[t] = mfma16(hfrag, tcol(Rd, t), f32x4{0.f, 0.f, 0.f, 0.f});
    w_epilogue(acc, &L.slab3[(0 * 4 + w) * 8 * SL_STRIDE], lane);
    if (lds_arrive(&L.ctrl[C_D3], lane) == 3) {
#pragma unroll
      for (int i = 0; i < 2; i++) {
        const int sub = (lane >> 3) + 8 * i, b = lane & 7;
        f32x4 s = slab_sum(&L.slab3[0], b, 4 * sub);
        st_sc1(rws, WS_FP + (u32)((ct_d * 16 + sub) * 32 + 16 + kb_d) * 128u + b * 16, canon(__builtin_bit_cast(u32x4, s)));
      }
    }
    if (w == 0) STAMP(14);
  }
  u32x4 ofrag;
  u32x4 Bo[8];
  {
    const int hq = 2 * kb_o + (w >> 1);
    const int b = lane & 7;
    const u32 oo = WS_OP + (u32)((hq * 8 + b) * 8) * 256u + (w & 1) * 128 + (16 * g + 8 * hi) * 2;
    const u32 so = WS_ST + (u32)((hq * 8 + b) * 8) * 8u;
    u32x4 od[8], sd[4];
    for (int it = 0; it < GLB_SPIN_CAP; it++) {
      CBAR();
#pragma unroll
      for (int i = 0; i < 4; i++) sd[i] = ld_sc1(rws, so + i * 16, 0);
#pragma unroll
      for (int s = 0; s < 8; s++) od[s] = ld_sc1(rws, oo + s * 256, 0);
      if (it == 0) {
#pragma unroll
        for (int t = 0; t < 8; t++) { Bo[t] = tcol(Ro, t); asm volatile("" : "+v"(Bo[t])); }
      }
      u32 mx = 0;
#pragma unroll
      for (int i = 0; i < 4; i++) mx = max(mx, umax4(sd[i]));
#pragma unroll
      for (int s = 0; s < 8; s++) mx = max(mx, umax4(od[s]));
      if (__all(mx != SENT)) break;
      __builtin_amdgcn_s_sleep(2);
    }
    CBAR();
    if (w == 0) STAMP(15);
    float M = -INFINITY;
#pragma unroll
    for (int i = 0; i < 4; i++) M = fmaxf(M, fmaxf(u2f(sd[i][0]), u2f(sd[i][2])));
    float wt[8];
    float Ls = 0.f;
#pragma unroll
    for (int i = 0; i < 4; i++) {
      wt[2 * i] = __expf(u2f(sd[i][0]) - M) * u2f(sd[i][1]);
      wt[2 * i + 1] = __expf(u2f(sd[i][2]) - M) * u2f(sd[i][3]);
      Ls += wt[2 * i] + wt[2 * i + 1];
    }
    const float inv = 1.f / Ls;
    float o[8];
#pragma unroll
    for (int i = 0; i < 8; i++) o[i] = 0.f;
#pragma unroll
    for (int s = 0; s < 8; s++) {
#pragma unroll
      for (int i = 0; i < 4; i++) {
        o[2 * i] += wt[s] * bflo(od[s][i]);
        o[2 * i + 1] += wt[s] * bfhi(od[s][i]);
      }
    }
    ofrag = u32x4{pack2(o[0] * inv, o[1] * inv), pack2(o[2] * inv, o[3] * inv), pack2(o[4] * inv, o[5] * inv), pack2(o[6] * inv, o[7] * inv)};
  }
  {
    f32x4 acc[8];
#pragma unroll
    for (int t = 0; t < 8; t++) acc[t] = mfma16(ofrag, Bo[t], f32x4{0.f, 0.f, 0.f, 0.f});
    w_epilogue(acc, &L.slab3[(1 * 4 + w) * 8 * SL_STRIDE], lane);
  }
  if (lds_arrive(&L.ctrl[C_O3], lane) != 3) return;
#pragma unroll
  for (int i = 0; i < 2; i++) {
    const int sub = (lane >> 3) + 8 * i, b = lane & 7;
    f32x4 s = slab_sum(&L.slab3[4 * 8 * SL_STRIDE], b, 4 * sub);
    st_sc1(rws, WS_FP + (u32)((ct_o * 16 + sub) * 32 + kb_o) * 128u + b * 16, canon(__builtin_bit_cast(u32x4, s)));
  }
  STAMP(16);
  {
    const u32 fo = WS_FP + (u32)c * 4096u + lane * 16;
    u32x4 f[4];
    for (int it = 0; it < GLB_SPIN_CAP; it++) {
      CBAR();
#pragma unroll
      for (int i = 0; i < 4; i++) f[i] = ld_sc1(rws, fo + i * 1024, 0);
      if (lane >= 48) f[3] = u32x4{0u, 0u, 0u, 0u};
      const u32 mx = max(max(umax4(f[0]), umax4(f[1])), max(umax4(f[2]), umax4(f[3])));
      if (__all(mx != SENT)) break;
      __builtin_amdgcn_s_sleep(1);
    }
    CBAR();
    STAMP(17);
    f32x4 S = __builtin_bit_cast(f32x4, f[0]) + __builtin_bit_cast(f32x4, f[1]);
    f32x4 Dn = __builtin_bit_cast(f32x4, f[2]) + __builtin_bit_cast(f32x4, f[3]);
    float r[4];
#pragma unroll
    for (int i = 0; i < 4; i++) {
      float s = S[i]; s += dpp<XOR8>(s); s = xrow_sum(s);
      float d = Dn[i]; d += dpp<XOR8>(d); d = xrow_sum(d);
      s = rbf(s); d = rbf(d);
      const float gg = rbf(d / (1.f + __expf(-d)));
      const float xv = (i & 1) ? bfhi(xfin[i >> 1]) : bflo(xfin[i >> 1]);
      r[i] = xv + rbf(s * gg);
    }
    if (lane < 8) *(u32x2*)&out[lane * 1024 + c * 4] = u32x2{pack2(r[0], r[1]), pack2(r[2], r[3])};
    STAMP(18);
  }
}

extern "C" void kernel_launch(void* const* d_in, const int* in_sizes, int n_in,
                              void* d_out, int out_size, void* d_ws, size_t ws_size,
                              hipStream_t stream) {
  fused_decode_kernel<<<256, 768, 0, stream>>>((const u16*)d_in[0], (const u16*)d_in[1], (const u16*)d_in[2],
                                               (const u16*)d_in[3], (const u16*)d_in[4], (const u16*)d_in[6],
                                               (const u16*)d_in[7], (const u16*)d_in[8], (u16*)d_out,
                                               (unsigned char*)d_ws);
}
```

```cpp
#include <hip/hip_runtime.h>
#include <stdint.h>

typedef unsigned int u32;
typedef unsigned long long u64;
typedef unsigned short u16;
typedef __attribute__((ext_vector_type(4))) unsigned int u32x4;
typedef __attribute__((ext_vector_type(2))) unsigned int u32x2;
typedef __attribute__((ext_vector_type(4))) float f32x4;
typedef __attribute__((ext_vector_type(8))) __bf16 bf16x8;
typedef __attribute__((ext_vector_type(2))) __bf16 bf16x2;
typedef __amdgpu_buffer_rsrc_t rsrc_t;

#define STAMPS 0

constexpr u32 KV_BYTES = 8u * 4u * 4096u * 128u * 2u;
constexpr u32 WQ_BYTES = 1024u * 4096u * 2u;
constexpr u32 WG_BYTES = 1024u * 3584u * 2u;

constexpr u32 WS_EPOCH = 0;
constexpr u32 WS_QP = 4096;
constexpr u32 WS_GP = WS_QP + 524288u;
constexpr u32 WS_OP = WS_GP + 112u * 4096u;
constexpr u32 WS_ST = WS_OP + 524288u;
constexpr u32 WS_FP = WS_ST + 32768u;
constexpr u32 WS_END = WS_FP + 256u * 8192u;

constexpr int AUX_NT = 2, AUX_SC1 = 16;
constexpr u32 SENT = 0xFFFFFFFFu;
constexpr u32 TMASK = 0x3FFu;
constexpr int LDS_SPIN_CAP = 1 << 21;
constexpr int GLB_SPIN_CAP = 1 << 17;

enum { C_LN = 0, C_CL, C_P1Q, C_P1G, C_P1I, C_X, C_Q, C_KVI, C_AQK, C_KVD, C_OP, C_D3, C_O3, C_G, C_N };

#define CBAR() asm volatile("" ::: "memory")
#define WIN 8
#define WIN1A 4
#define WIN1 WIN
#define WIN3 WIN
#define PSLEEP 4
template <int N> __device__ __forceinline__ void wait_vm() { asm volatile("s_waitcnt vmcnt(%0)" ::"n"(N) : "memory"); }

__device__ __forceinline__ rsrc_t mk_rsrc(const void* p, u32 bytes) {
  return __builtin_amdgcn_make_buffer_rsrc((void*)p, (short)0, (int)bytes, 0x00020000);
}
__device__ __forceinline__ u32x4 ld_nt(rsrc_t r, u32 voff, u32 soff) { return __builtin_amdgcn_raw_buffer_load_b128(r, (int)voff, (int)soff, AUX_NT); }
__device__ __forceinline__ u32x4 ld_sc1(rsrc_t r, u32 voff, u32 soff) { return __builtin_amdgcn_raw_buffer_load_b128(r, (int)voff, (int)soff, AUX_SC1); }
__device__ __forceinline__ void st_sc1(rsrc_t r, u32 voff, u32x4 v) { __builtin_amdgcn_raw_buffer_store_b128(v, r, (int)voff, 0, AUX_SC1); }
template <int OFF> __device__ __forceinline__ void dma16(u32x4 srd, u32 voff, u32 soff, u32 ldsaddr) {
  u32 keep;
  asm volatile("s_mov_b32 %0, m0\n\ts_mov_b32 m0, %4\n\ts_nop 0\n\tbuffer_load_dwordx4 %1, %2, %3 offen offset:%5 nt lds\n\ts_mov_b32 m0, %0"
               : "=&s"(keep) : "v"(voff), "s"(srd), "s"(soff), "s"(ldsaddr), "n"(OFF) : "memory");
}

__device__ __forceinline__ float u2f(u32 x) { return __uint_as_float(x); }
__device__ __forceinline__ u32 f2u(float x) { return __float_as_uint(x); }
__device__ __forceinline__ float bflo(u32 x) { return u2f(x << 16); }
__device__ __forceinline__ float bfhi(u32 x) { return u2f(x & 0xFFFF0000u); }
__device__ __forceinline__ u32 pack2(float lo, float hi) {
  bf16x2 v = {(__bf16)lo, (__bf16)hi};
  return __builtin_bit_cast(u32, v);
}
__device__ __forceinline__ float rbf(float x) { return (float)(__bf16)x; }
template <int CTRL> __device__ __forceinline__ float dpp(float x) {
  return __builtin_bit_cast(float, __builtin_amdgcn_mov_dpp(__builtin_bit_cast(int, x), CTRL, 0xf, 0xf, true));
}
constexpr int XOR1 = 0xB1, XOR2 = 0x4E, XOR7 = 0x141, XOR8 = 0x128;
__device__ __forceinline__ float x32_max(float x) {
  auto t = __builtin_amdgcn_permlane32_swap(f2u(x), f2u(x), false, false);
  return fmaxf(u2f(t[0]), u2f(t[1]));
}
__device__ __forceinline__ float x32_sum(float x) {
  auto t = __builtin_amdgcn_permlane32_swap(f2u(x), f2u(x), false, false);
  return u2f(t[0]) + u2f(t[1]);
}
__device__ __forceinline__ float xrow_max(float x) {
  auto s = __builtin_amdgcn_permlane16_swap(f2u(x), f2u(x), false, false);
  return x32_max(fmaxf(u2f(s[0]), u2f(s[1])));
}
__device__ __forceinline__ float xrow_sum(float x) {
  auto s = __builtin_amdgcn_permlane16_swap(f2u(x), f2u(x), false, false);
  return x32_sum(u2f(s[0]) + u2f(s[1]));
}
__device__ __forceinline__ float wave_sum(float x) {
  x += dpp<XOR1>(x); x += dpp<XOR2>(x); x += dpp<XOR7>(x); x += dpp<XOR8>(x);
  return xrow_sum(x);
}
__device__ __forceinline__ f32x4 mfma16(u32x4 a, u32x4 b, f32x4 c) {
  return __builtin_amdgcn_mfma_f32_16x16x32_bf16(__builtin_bit_cast(bf16x8, a), __builtin_bit_cast(bf16x8, b), c, 0, 0, 0);
}
__device__ __forceinline__ u32x4 tcol(const u32x4* R, int t) {
  const u32 sel = (t & 1) ? 0x07060302u : 0x05040100u;
  const int d = t >> 1;
  u32x4 r;
  r[0] = __builtin_amdgcn_perm(R[1][d], R[0][d], sel);
  r[1] = __builtin_amdgcn_perm(R[3][d], R[2][d], sel);
  r[2] = __builtin_amdgcn_perm(R[5][d], R[4][d], sel);
  r[3] = __builtin_amdgcn_perm(R[7][d], R[6][d], sel);
  return r;
}
__device__ __forceinline__ u32 umax3(u32 a, u32 b, u32 c) { return max(max(a, b), c); }
__device__ __forceinline__ u32 umax4(u32x4 v) { return max(umax3(v[0], v[1], v[2]), v[3]); }
__device__ __forceinline__ u32x4 canon(u32x4 v) {
  v[0] = min(v[0], 0xFFFFFFFEu); v[1] = min(v[1], 0xFFFFFFFEu); v[2] = min(v[2], 0xFFFFFFFEu); v[3] = min(v[3], 0xFFFFFFFEu);
  return v;
}
__device__ __forceinline__ u32x4 enc(f32x4 v, u32x4 T) {
  u32x4 r;
#pragma unroll
  for (int i = 0; i < 4; i++) {
    u32 u = f2u(v[i]);
    u = ((u & 0x7FFFFFFFu) >= 0x7F800000u) ? u : u + 0x200u;
    r[i] = (u & ~TMASK) | (T[i] & TMASK);
  }
  return r;
}
__device__ __forceinline__ f32x4 dec(u32x4 w) {
  return f32x4{u2f(w[0] & ~TMASK), u2f(w[1] & ~TMASK), u2f(w[2] & ~TMASK), u2f(w[3] & ~TMASK)};
}
__device__ __forceinline__ u32 tdiff(u32x4 w, u32x4 T) { return ((w[0] ^ T[0]) | (w[1] ^ T[1])) | ((w[2] ^ T[2]) | (w[3] ^ T[3])); }

template <int SL = 1> __device__ __forceinline__ void lds_wait(u32* p, u32 target) {
  CBAR();
#pragma nounroll
  for (int i = 0; i < LDS_SPIN_CAP; i++) {
    u32 v = __hip_atomic_load(p, __ATOMIC_RELAXED, __HIP_MEMORY_SCOPE_WORKGROUP);
    if (__builtin_amdgcn_readfirstlane(v) >= target) break;
    __builtin_amdgcn_s_sleep(SL);
  }
  CBAR();
}
__device__ __forceinline__ u32 lds_arrive(u32* p, int lane) {
  asm volatile("s_waitcnt lgkmcnt(0)" ::: "memory");
  u32 old = 0;
  if (lane == 0) old = __hip_atomic_fetch_add(p, 1u, __ATOMIC_RELAXED, __HIP_MEMORY_SCOPE_WORKGROUP);
  old = __builtin_amdgcn_readfirstlane(old);
  CBAR();
  return old;
}
__device__ __forceinline__ void lds_signal(u32* p, int lane) {
  asm volatile("s_waitcnt lgkmcnt(0)" ::: "memory");
  if (lane == 0) __hip_atomic_fetch_add(p, 1u, __ATOMIC_RELAXED, __HIP_MEMORY_SCOPE_WORKGROUP);
  CBAR();
}

constexpr int XN_STRIDE = 1024 + 8;
constexpr int SL_STRIDE = 68;
constexpr int S3_STRIDE = 136;
constexpr int Q_STRIDE = 136;
constexpr int OV_XN = 65536, OV_SQ = OV_XN + 8 * XN_STRIDE * 2, OV_SG = OV_SQ + 4 * 8 * SL_STRIDE * 4;
static_assert(OV_SG + 4 * 8 * SL_STRIDE * 4 <= 131072, "overlay");
constexpr int OV_S3 = 4096;

struct __align__(16) Lds {
  unsigned char kimg[131072];
  float kvst[8 * 8 * 2];
  u16 qs[8 * Q_STRIDE];
  u16 oscr[4 * 8 * 32];
  float gate[32];
  u32 ctrl[16];
  u32 tag[4];
};

__device__ __forceinline__ void w_epilogue(const f32x4* acc, float* slab, int lane) {
  float val[2][8];
#pragma unroll
  for (int t = 0; t < 8; t++) {
#pragma unroll
    for (int r = 0; r < 2; r++) {
      auto s = __builtin_amdgcn_permlane32_swap(f2u(acc[t][r]), f2u(acc[t][r + 2]), false, false);
      val[r][t] = u2f(s[0]) + dpp<XOR8>(u2f(s[1]));
    }
  }
  if (!(lane & 8)) {
    const int b0 = 4 * ((lane >> 4) & 1) + 2 * (lane >> 5);
    const int cp = lane & 7;
#pragma unroll
    for (int r = 0; r < 2; r++) {
      float* p = slab + (b0 + r) * SL_STRIDE + 8 * cp;
      *(f32x4*)p = f32x4{val[r][0], val[r][1], val[r][2], val[r][3]};
      *(f32x4*)(p + 4) = f32x4{val[r][4], val[r][5], val[r][6], val[r][7]};
    }
  }
}
__device__ __forceinline__ f32x4 slab_sum(const float* slab, int b, int col) {
  const float* p = slab + b * SL_STRIDE + col;
  f32x4 a = *(const f32x4*)p;
  f32x4 bb = *(const f32x4*)(p + 8 * SL_STRIDE);
  f32x4 c = *(const f32x4*)(p + 16 * SL_STRIDE);
  f32x4 d = *(const f32x4*)(p + 24 * SL_STRIDE);
  return (a + bb) + (c + d);
}
__device__ __forceinline__ void w3_store(const f32x4* acc, float* slab, int lane) {
  if (lane < 32) {
#pragma unroll
    for (int r = 0; r < 4; r++) {
      float* p = slab + (4 * (lane >> 4) + r) * S3_STRIDE + (lane & 15) * 4;
      *(f32x4*)p = f32x4{acc[0][r], acc[1][r], acc[2][r], acc[3][r]};
      *(f32x4*)(p + 64) = f32x4{acc[4][r], acc[5][r], acc[6][r], acc[7][r]};
    }
  }
}
__device__ __forceinline__ f32x4 w3_sum(const unsigned char* kimg, int mat, int sub, int b) {
  const float* p = (const float*)(kimg + OV_S3) + mat * 8 * S3_STRIDE + b * S3_STRIDE + (sub & 1) * 64 + (sub >> 1) * 4;
  f32x4 a = *(const f32x4*)p;
  f32x4 bb = *(const f32x4*)(p + 4096);
  f32x4 c = *(const f32x4*)(p + 8192);
  f32x4 d = *(const f32x4*)(p + 12288);
  return (a + bb) + (c + d);
}
__device__ __forceinline__ u32x4 read_tag(const u32* p) {
  u32x4 t = *(const u32x4*)p;
  t[0] = __builtin_amdgcn_readfirstlane(t[0]); t[1] = __builtin_amdgcn_readfirstlane(t[1]);
  t[2] = __builtin_amdgcn_readfirstlane(t[2]); t[3] = __builtin_amdgcn_readfirstlane(t[3]);
  return t;
}

#define STAMP(i) do { } while (0)

__global__ void __launch_bounds__(768, 3)
fused_decode_kernel(const u16* x, const u16* kc, const u16* vc,
                    const u16* wq, const u16* wo, const u16* wg,
                    const u16* wu, const u16* wd, u16* out,
                    unsigned char* ws) {
  __shared__ Lds L;
  const int tid = threadIdx.x;
  const int lane = tid & 63;
  const int wave = __builtin_amdgcn_readfirstlane(tid >> 6);
  const int c = blockIdx.x;
  const int hkv = c >> 6, gi = c & 63;
  const int ab = gi >> 3, as = gi & 7;
  const bool mlp = c < 224;
  const rsrc_t rws = mk_rsrc(ws, WS_END);
  u16* xn = (u16*)(L.kimg + OV_XN);
  const int g = lane >> 4;

  if (wave >= 4) {
    const int v = wave - 4;
    const bool grpB = v >= 4;
    if (v == 0) STAMP(0);
    const rsrc_t rx = mk_rsrc(x, 8u * 1024u * 2u);
    u32x4 xa = __builtin_amdgcn_raw_buffer_load_b128(rx, v * 2048 + lane * 16, 0, 0);
    u32x4 xb = __builtin_amdgcn_raw_buffer_load_b128(rx, v * 2048 + 1024 + lane * 16, 0, 0);
    const int ct_q = hkv * 16 + (gi >> 2), ks_q = gi & 3;
    u32x4 Rq[8];
    u32x4 ep = {0u, 0u, 0u, 0u};
    u32x2 xfin = {0u, 0u};
    if (grpB) {
      const rsrc_t rq = mk_rsrc(wq, WQ_BYTES);
      const u32 qoff = (u32)(16 * g + 8 * ((lane >> 3) & 1)) * 8192u + (lane & 7) * 16;
      const u32 qbase = (u32)(ks_q * 256 + (v - 4) * 64) * 8192u + ct_q * 128;
#pragma unroll
      for (int i = 0; i < 8; i++) Rq[i] = ld_nt(rq, qoff, qbase + i * 8192);
    } else {
      if (v == 0) {
        ep = ld_sc1(rws, WS_EPOCH, 0);
        xfin = __builtin_amdgcn_raw_buffer_load_b64(rx, (lane & 7) * 2048 + c * 8, 0, 0);
      }
      if (lane < 32) {
        const int h = hkv * 8 + 2 * v + (lane >> 4), kq = (lane >> 2) & 3;
        st_sc1(rws, WS_OP + (u32)((((h * 4 + kq) * 8 + as) * 8 + ab) * 64 + (lane & 3) * 16), u32x4{SENT, SENT, SENT, SENT});
      }
      if (v == 1) {
        if (lane < 16) L.ctrl[lane] = 0;
        asm volatile("s_waitcnt lgkmcnt(0)" ::: "memory");
      }
    }
    __builtin_amdgcn_sched_barrier(0);
    CBAR();
    __builtin_amdgcn_s_barrier();
    CBAR();
    __builtin_amdgcn_s_setprio(2);
    {
      float e[16];
#pragma unroll
      for (int i = 0; i < 4; i++) { e[2 * i] = bflo(xa[i]); e[2 * i + 1] = bfhi(xa[i]); e[8 + 2 * i] = bflo(xb[i]); e[9 + 2 * i] = bfhi(xb[i]); }
      float s = 0.f;
#pragma unroll
      for (int i = 0; i < 16; i++) s += e[i];
      const float mean = wave_sum(s) * (1.f / 1024.f);
      float s2 = 0.f;
#pragma unroll
      for (int i = 0; i < 16; i++) { float d = e[i] - mean; s2 += d * d; }
      const float var = wave_sum(s2) * (1.f / 1024.f);
      const float mb = rbf(mean), vb = rbf(var);
      const float den = rbf(sqrtf(rbf(vb + 1e-5f)));
      u32x4 ya, yb;
#pragma unroll
      for (int i = 0; i < 4; i++) {
        ya[i] = pack2(rbf(e[2 * i] - mb) / den, rbf(e[2 * i + 1] - mb) / den);
        yb[i] = pack2(rbf(e[8 + 2 * i] - mb) / den, rbf(e[9 + 2 * i] - mb) / den);
      }
      *(u32x4*)&xn[v * XN_STRIDE + lane * 8] = ya;
      *(u32x4*)&xn[v * XN_STRIDE + 512 + lane * 8] = yb;
    }
    lds_signal(&L.ctrl[C_LN], lane);
    if (!grpB) {
      if (v == 0) {
        u64 a = ((u64)ep[1] << 32) | ep[0], b = ((u64)ep[3] << 32) | ep[2];
        u64 h1 = (a ^ (b * 0x9E3779B97F4A7C15ull)) * 6364136223846793005ull + 1442695040888963407ull;
        h1 ^= h1 >> 29; h1 *= 0xBF58476D1CE4E5B9ull; h1 ^= h1 >> 32;
        u64 h2 = (h1 + b) * 0x94D049BB133111EBull + 0x2545F4914F6CDD1Dull;
        h2 ^= h2 >> 31;
        u32x4 H = {(u32)h1, (u32)(h1 >> 32), (u32)h2, (u32)(h2 >> 32)};
        u32x4 T;
#pragma unroll
        for (int i = 0; i < 4; i++) T[i] = (H[i] & ~TMASK) | ((ep[i] + 1u + (H[i] & 0x1FFu)) & TMASK);
        if (lane == 0) *(u32x4*)L.tag = T;
      }
      asm volatile("s_waitcnt vmcnt(0)" ::: "memory");
      lds_signal(&L.ctrl[C_CL], lane);
      if (v == 0) STAMP(1);
      __builtin_amdgcn_s_setprio(0);
      lds_wait(&L.ctrl[C_P1I], 4);
    } else {
      lds_wait(&L.ctrl[C_LN], 8);
      const int r = lane & 15;
      const u32x4 af = *(const u32x4*)&xn[(r & 7) * XN_STRIDE + 16 * g + 8 * (r >> 3) + ks_q * 256 + (v - 4) * 64];
      f32x4 acc[8];
#pragma unroll
      for (int t = 0; t < 8; t++) acc[t] = mfma16(af, tcol(Rq, t), f32x4{0.f, 0.f, 0.f, 0.f});
      float* sq = (float*)(L.kimg + OV_SQ);
      w_epilogue(acc, sq + (v - 4) * 8 * SL_STRIDE, lane);
      if (lds_arrive(&L.ctrl[C_P1Q], lane) == 3) {
        STAMP(6);
        f32x4 s0 = slab_sum(sq, g, 4 * (lane & 15)), s1 = slab_sum(sq, g + 4, 4 * (lane & 15));
        lds_wait(&L.ctrl[C_CL], 4);
        const u32x4 T = read_tag(L.tag);
        const u32 qo = WS_QP + (u32)((g * 4 + ks_q) * 4096 + ct_q * 64 + 4 * (lane & 15)) * 4u;
        st_sc1(rws, qo, enc(s0, T));
        st_sc1(rws, qo + 4u * 4u * 4096u * 4u, enc(s1, T));
        lds_signal(&L.ctrl[C_X], lane);
        STAMP(7);
      }
      __builtin_amdgcn_s_setprio(0);
      lds_wait<4>(&L.ctrl[C_X], 2);
      lds_wait(&L.ctrl[C_KVI], 4);
    }
    const u32 kvbase = (u32)((ab * 4 + hkv) * 4096 + as * 512 + v * 64) * 256u;
    const int rho = lane & 15;
    unsigned char* kreg = L.kimg + v * 16384;
    {
      const u64 kp = (u64)kc;
      const u32x4 srd = {(u32)kp, (u32)(kp >> 32) & 0xFFFFu, KV_BYTES, 0x00020000u};
      const u32 lbase = (u32)(uintptr_t)(__attribute__((address_space(3))) unsigned char*)kreg;
      u32 ko[4];
#pragma unroll
      for (int a = 0; a < 4; a++)
        ko[a] = (u32)(g * 256 + ((rho ^ (4 * g + a)) * 16));
#pragma unroll
      for (int T = 0; T < 4; T++) {
        if (T * 4 + 0 >= WIN) wait_vm<WIN - 1>();
        dma16<0>(srd, ko[0], kvbase + T * 4096, lbase + T * 4096);
        if (T * 4 + 1 >= WIN) wait_vm<WIN - 1>();
        dma16<1024>(srd, ko[1], kvbase + T * 4096, lbase + T * 4096);
        if (T * 4 + 2 >= WIN) wait_vm<WIN - 1>();
        dma16<2048>(srd, ko[2], kvbase + T * 4096, lbase + T * 4096);
        if (T * 4 + 3 >= WIN) wait_vm<WIN - 1>();
        dma16<3072>(srd, ko[3], kvbase + T * 4096, lbase + T * 4096);
      }
    }
    u32x4 V[2][8];
    {
      const rsrc_t rv = mk_rsrc(vc, KV_BYTES);
#pragma unroll
      for (int u = 0; u < 2; u++)
#pragma unroll
        for (int i = 0; i < 8; i++) {
          wait_vm<WIN - 1>();
          V[u][i] = ld_nt(rv, lane * 16 + (i & 3) * 1024, kvbase + u * 8192 + (i >> 2) * 4096);
        }
    }
    __builtin_amdgcn_sched_barrier(0);
    lds_signal(&L.ctrl[C_KVI], lane);
    if (v == 7) STAMP(2);
    if (v == 3) STAMP(19);
    lds_wait(&L.ctrl[C_Q], 4);
    __builtin_amdgcn_s_setprio(2);
    wait_vm<16>();
    if (v == 0) STAMP(3);
    f32x4 sa[4];
#pragma unroll
    for (int T = 0; T < 4; T++) sa[T] = f32x4{0.f, 0.f, 0.f, 0.f};
    {
      const unsigned char* kr = kreg + (4 * (rho & 3) + (rho >> 2)) * 256;
#pragma unroll
      for (int st = 0; st < 4; st++) {
        const u32x4 qf = *(const u32x4*)&L.qs[(lane & 7) * Q_STRIDE + st * 32 + g * 8];
        const unsigned char* p = kr + (((4 * st + g) ^ rho) * 16);
#pragma unroll
        for (int T = 0; T < 4; T++) sa[T] = mfma16(*(const u32x4*)(p + T * 4096), qf, sa[T]);
      }
    }
    float sc[16];
    float m = -INFINITY;
#pragma unroll
    for (int T = 0; T < 4; T++)
#pragma unroll
      for (int r = 0; r < 4; r++) { float z = rbf(rbf(sa[T][r]) * 0.08838834764831845f); sc[T * 4 + r] = z; m = fmaxf(m, z); }
    if (!grpB) lds_signal(&L.ctrl[C_AQK], lane);
    m = xrow_max(m);
    float lsum = 0.f;
#pragma unroll
    for (int i = 0; i < 16; i++) { sc[i] = __expf(sc[i] - m); lsum += sc[i]; }
    lsum = xrow_sum(lsum);
    if (lane < 8) { L.kvst[(lane * 8 + v) * 2] = m; L.kvst[(lane * 8 + v) * 2 + 1] = lsum; }
    f32x4 oa[8];
#pragma unroll
    for (int t = 0; t < 8; t++) oa[t] = f32x4{0.f, 0.f, 0.f, 0.f};
#pragma unroll
    for (int u = 0; u < 2; u++) {
      u32x4 pf;
#pragma unroll
      for (int i = 0; i < 4; i++) pf[i] = pack2(sc[8 * u + 2 * i], sc[8 * u + 2 * i + 1]);
#pragma unroll
      for (int t = 0; t < 8; t++) oa[t] = mfma16(pf, tcol(V[u], t), oa[t]);
    }
    if (g < 2) {
#pragma unroll
      for (int r = 0; r < 4; r++) {
        float* p = (float*)kreg + (4 * g + r) * 128 + rho * 4;
        *(f32x4*)p = f32x4{oa[0][r], oa[1][r], oa[2][r], oa[3][r]};
        *(f32x4*)(p + 64) = f32x4{oa[4][r], oa[5][r], oa[6][r], oa[7][r]};
      }
    }
    lds_signal(&L.ctrl[C_KVD], lane);
    const u32x4 T = read_tag(L.tag);
    lds_wait(&L.ctrl[C_KVD], 8);
    if (v == 0) STAMP(4);
    {
      const f32x4* sp = (const f32x4*)&L.kvst[v * 16];
      const f32x4 s0 = sp[0], s1 = sp[1], s2 = sp[2], s3 = sp[3];
      const f32x4 sm = sp[g];
      const float* p0 = (const float*)(L.kimg + (2 * g) * 16384) + v * 128 + rho * 4;
      const float* p1 = p0 + 4096;
      f32x4 a0 = *(const f32x4*)p0, a1 = *(const f32x4*)(p0 + 64), b0 = *(const f32x4*)p1, b1 = *(const f32x4*)(p1 + 64);
      const float M = fmaxf(fmaxf(fmaxf(s0[0], s0[2]), fmaxf(s1[0], s1[2])), fmaxf(fmaxf(s2[0], s2[2]), fmaxf(s3[0], s3[2])));
      const float e0 = __expf(sm[0] - M), e1 = __expf(sm[2] - M);
      const float Ls = xrow_sum(e0 * sm[1] + e1 * sm[3]);
      const float inv = 1.f / Ls;
      float o[8];
#pragma unroll
      for (int i = 0; i < 4; i++) {
        o[i] = xrow_sum(e0 * a0[i] + e1 * b0[i]) * inv;
        o[4 + i] = xrow_sum(e0 * a1[i] + e1 * b1[i]) * inv;
      }
      u32x4 ov = {pack2(o[0], o[1]), pack2(o[2], o[3]), pack2(o[4], o[5]), pack2(o[6], o[7])};
      ov = canon(ov);
      const int h = hkv * 8 + v;
      u32 off = WS_OP + (u32)((((h * 4 + (rho >> 2)) * 8 + as) * 8 + ab) * 64 + (rho & 3) * 16);
      if (lane == 16) { off = WS_ST + (u32)(((h * 8 + ab) * 8 + as) * 16); ov = u32x4{f2u(M), f2u(Ls), T[2], T[3]}; }
      if (lane <= 16) st_sc1(rws, off, ov);
    }
    lds_signal(&L.ctrl[C_OP], lane);
    if (v == 0) STAMP(5);
    if (v >= 2) return;
    {
      if (v == 0) lds_wait(&L.ctrl[C_O3], 4);
      else if (mlp) lds_wait<4>(&L.ctrl[C_D3], 4);
      const u32 fo = WS_FP + (u32)c * 8192u + v * 4096 + lane * 16;
      u32x4 f[4];
      bool need = true;
#pragma nounroll
      for (int it = 0; it < GLB_SPIN_CAP; it++) {
        CBAR();
        if (need) {
#pragma unroll
          for (int i = 0; i < 4; i++) f[i] = ld_sc1(rws, fo + i * 1024, 0);
        }
        if (v == 1 && lane >= 32) f[3] = u32x4{T[0] & TMASK, T[1] & TMASK, T[2] & TMASK, T[3] & TMASK};
        need = ((tdiff(f[0], T) | tdiff(f[1], T) | tdiff(f[2], T) | tdiff(f[3], T)) & TMASK) != 0;
        if (!__any(need)) break;
        __builtin_amdgcn_s_sleep(1);
      }
      CBAR();
      if (v == 0) STAMP(17);
      f32x4 S = (dec(f[0]) + dec(f[1])) + (dec(f[2]) + dec(f[3]));
#pragma unroll
      for (int i = 0; i < 4; i++) { float s = S[i]; s += dpp<XOR8>(s); S[i] = rbf(xrow_sum(s)); }
      if (v == 1) {
        f32x4 G;
#pragma unroll
        for (int i = 0; i < 4; i++) G[i] = rbf(S[i] / (1.f + __expf(-S[i])));
        if (lane < 8) *(f32x4*)&L.gate[lane * 4] = G;
        lds_signal(&L.ctrl[C_G], lane);
        STAMP(14);
        return;
      }
      lds_wait(&L.ctrl[C_G], 1);
      const f32x4 G = *(const f32x4*)&L.gate[(lane & 7) * 4];
      float r[4];
#pragma unroll
      for (int i = 0; i < 4; i++) {
        const float xv = (i & 1) ? bfhi(xfin[i >> 1]) : bflo(xfin[i >> 1]);
        r[i] = xv + rbf(S[i] * G[i]);
      }
      if (lane < 8) *(u32x2*)&out[lane * 1024 + c * 4] = u32x2{pack2(r[0], r[1]), pack2(r[2], r[3])};
      if (c == 0 && lane == 0) st_sc1(rws, WS_EPOCH, T);
      STAMP(18);
    }
    return;
  }

  const int w = wave;
  const int kb_d = c >> 3, ct_d = c & 7;
  const int h_o = hkv * 8 + (gi >> 3), ct_o = gi & 7;
  {
    const int mat = c >= 112 ? 1 : 0;
    const int mm = c - 112 * mat;
    const int ct_g = mm >> 1, ks_g = mm & 1;
    u32x4 Rg[2][8];
    const rsrc_t rg = mk_rsrc(mat ? wu : wg, mlp ? WG_BYTES : 0u);
    const u32 goff = (u32)(16 * g + 8 * ((lane >> 3) & 1)) * 7168u + (lane & 7) * 16;
    const u32 gbase = (u32)(ks_g * 512 + w * 128) * 7168u + ct_g * 128;
#pragma unroll
    for (int n = 0; n < 16; n++) {
      if (n == WIN1A) { __builtin_amdgcn_sched_barrier(0); CBAR(); __builtin_amdgcn_s_barrier(); CBAR(); }
      if (n >= WIN1A && n < 8) wait_vm<WIN1A - 1>();
      if (n >= 8 && n >= WIN1) wait_vm<WIN1 - 1>();
      Rg[n >> 3][n & 7] = ld_nt(rg, goff, gbase + ((n >> 3) * 64 + (n & 7)) * 7168);
    }
    __builtin_amdgcn_sched_barrier(0);
    lds_signal(&L.ctrl[C_P1I], lane);
    if (w == 0) STAMP(8);
    lds_wait(&L.ctrl[C_LN], 8);
    const int r = lane & 15;
    const u16* xrow = &xn[(r & 7) * XN_STRIDE + 16 * g + 8 * (r >> 3) + ks_g * 512 + w * 128];
    const u32x4 a0 = *(const u32x4*)xrow;
    const u32x4 a1 = *(const u32x4*)(xrow + 64);
    f32x4 acc[8];
#pragma unroll
    for (int t = 0; t < 8; t++) acc[t] = mfma16(a0, tcol(Rg[0], t), f32x4{0.f, 0.f, 0.f, 0.f});
#pragma unroll
    for (int t = 0; t < 8; t++) acc[t] = mfma16(a1, tcol(Rg[1], t), acc[t]);
    float* sg = (float*)(L.kimg + OV_SG);
    w_epilogue(acc, sg + w * 8 * SL_STRIDE, lane);
    const u32 old = lds_arrive(&L.ctrl[C_P1G], lane);
    lds_wait(&L.ctrl[C_CL], 4);
    if (old == 3) {
      STAMP(9);
      const u32x4 T = read_tag(L.tag);
      const int b = lane & 7, hg = lane >> 3;
#pragma unroll
      for (int i = 0; i < 2; i++) {
        f32x4 s = slab_sum(sg, b, i * 32 + 4 * hg);
        if (mlp) st_sc1(rws, WS_GP + (u32)(((((ct_g * 2 + i) * 2 + ks_g) * 8 + hg) * 2 + mat) * 128 + b * 16), enc(s, T));
      }
      lds_signal(&L.ctrl[C_X], lane);
      STAMP(10);
    }
  }
  const u32x4 T = read_tag(L.tag);
  {
    const u32 qo = WS_QP + (u32)(ab * 4 * 4096 + hkv * 1024 + w * 256 + 4 * lane) * 4u;
    u32x4 q[4];
    bool need = true;
#pragma nounroll
    for (int it = 0; it < GLB_SPIN_CAP; it++) {
      CBAR();
      if (need) {
#pragma unroll
        for (int i = 0; i < 4; i++)
          q[i] = ld_sc1(rws, qo, i * 16384);
      }
      need = ((tdiff(q[0], T) | tdiff(q[1], T) | tdiff(q[2], T) | tdiff(q[3], T)) & TMASK) != 0;
      if (!__any(need)) break;
      __builtin_amdgcn_s_sleep(PSLEEP);
    }
    CBAR();
    const f32x4 qs = (dec(q[0]) + dec(q[1])) + (dec(q[2]) + dec(q[3]));
    *(u32x2*)&L.qs[(2 * w + (lane >> 5)) * Q_STRIDE + ((4 * lane) & 127)] = u32x2{pack2(qs[0], qs[1]), pack2(qs[2], qs[3])};
    lds_signal(&L.ctrl[C_Q], lane);
    if (w == 0) STAMP(12);
  }
  u32x4 hfrag = {0u, 0u, 0u, 0u};
  if (mlp) {
    const u32 ho = WS_GP + (u32)(kb_d * 4 + w) * 4096u + lane * 16;
    u32x4 p[4];
    bool need = true;
#pragma nounroll
    for (int it = 0; it < GLB_SPIN_CAP; it++) {
      CBAR();
      if (need) {
#pragma unroll
        for (int i = 0; i < 4; i++)
          p[i] = ld_sc1(rws, ho + i * 1024, 0);
      }
      need = ((tdiff(p[0], T) | tdiff(p[1], T) | tdiff(p[2], T) | tdiff(p[3], T)) & TMASK) != 0;
      if (!__any(need)) break;
      __builtin_amdgcn_s_sleep(PSLEEP);
    }
    CBAR();
    const bool isup = (lane >> 3) & 1;
    float hv[8];
#pragma unroll
    for (int hf = 0; hf < 2; hf++) {
      const f32x4 S = dec(p[hf]) + dec(p[2 + hf]);
#pragma unroll
      for (int i = 0; i < 4; i++) {
        const float own = rbf(S[i]);
        const float oth = dpp<XOR8>(own);
        const float gg = isup ? oth : own, uu = isup ? own : oth;
        const float sg = rbf(1.f / (1.f + __expf(-gg)));
        hv[hf * 4 + i] = sg * uu;
      }
    }
    hfrag = u32x4{pack2(hv[0], hv[1]), pack2(hv[2], hv[3]), pack2(hv[4], hv[5]), pack2(hv[6], hv[7])};
  }
  if (w == 0) STAMP(11);
  lds_wait(&L.ctrl[C_KVI], 8);
  if (w == 0) STAMP(13);
  u32x4 Rd[8], Ro[8];
  {
    const int cp = lane & 15;
    const rsrc_t rd = mk_rsrc(wd, mlp ? WG_BYTES : 0u);
    const u32 doff = (u32)(4 * g) * 2048u + cp * 16;
    const u32 dbase = (u32)(kb_d * 128 + w * 32) * 2048u + ct_d * 256;
#pragma unroll
    for (int i = 0; i < 8; i++) { if (i >= WIN3) wait_vm<WIN3 - 1>(); Rd[i] = ld_nt(rd, doff, dbase + (16 * (i >> 2) + (i & 3)) * 2048); }
    const rsrc_t ro = mk_rsrc(wo, WQ_BYTES);
    const u32 ooff = (u32)(8 * g) * 2048u + cp * 16;
    const u32 obase = (u32)(h_o * 128 + w * 32) * 2048u + ct_o * 256;
#pragma unroll
    for (int i = 0; i < 8; i++) { if (8 + i >= WIN3) wait_vm<WIN3 - 1>(); Ro[i] = ld_nt(ro, ooff, obase + i * 2048); }
  }
  __builtin_amdgcn_sched_barrier(0);
  __builtin_amdgcn_s_setprio(1);
  float* s3 = (float*)(L.kimg + OV_S3 + w * 16384);
  lds_wait(&L.ctrl[C_AQK], 4);
  if (mlp) {
    f32x4 acc[8];
#pragma unroll
    for (int t = 0; t < 8; t++) acc[t] = mfma16(hfrag, tcol(Rd, t), f32x4{0.f, 0.f, 0.f, 0.f});
    w3_store(acc, s3, lane);
    if (lds_arrive(&L.ctrl[C_D3], lane) == 3) {
#pragma unroll
      for (int i = 0; i < 4; i++) {
        const int sub = (lane >> 3) + 8 * i, b = lane & 7;
        st_sc1(rws, WS_FP + (u32)(((ct_d * 32 + sub) * 64 + 32 + kb_d) * 128 + b * 16), enc(w3_sum(L.kimg, 0, sub, b), T));
      }
    }
  }
  u32x4 Bo[8];
#pragma unroll
  for (int t = 0; t < 8; t++) { Bo[t] = tcol(Ro, t); asm volatile("" : "+v"(Bo[t])); }
  if (w == 0) STAMP(20);
  lds_wait(&L.ctrl[C_OP], 8);
  __builtin_amdgcn_s_setprio(2);
  u32x4 ofrag;
  {
    const int sh = lane >> 5, b = (lane >> 2) & 7, ch = lane & 3;
    const u32 oo = WS_OP + (u32)(((h_o * 4 + w) * 8 + 4 * sh) * 512 + (lane & 31) * 16);
    const u32 so = WS_ST + (u32)(((h_o * 8 + b) * 8 + 4 * sh + ch) * 16);
    u32x4 od[4], sd;
    bool need = true;
#pragma nounroll
    for (int it = 0; it < GLB_SPIN_CAP; it++) {
      CBAR();
      if (need) {
        sd = ld_sc1(rws, so, 0);
#pragma unroll
        for (int i = 0; i < 4; i++) od[i] = ld_sc1(rws, oo + i * 512, 0);
      }
      const u32 mx = max(max(umax4(od[0]), umax4(od[1])), max(umax4(od[2]), umax4(od[3])));
      need = (mx == SENT) || (((sd[2] ^ T[2]) | (sd[3] ^ T[3])) != 0);
      if (!__any(need)) break;
      __builtin_amdgcn_s_sleep(1);
    }
    CBAR();
    if (w == 0) STAMP(15);
    const float mm = u2f(sd[0]);
    float M = fmaxf(mm, dpp<XOR1>(mm));
    M = fmaxf(M, dpp<XOR2>(M));
    M = x32_max(M);
    const float wt = __expf(mm - M) * u2f(sd[1]);
    float Ls = wt + dpp<XOR1>(wt);
    Ls += dpp<XOR2>(Ls);
    Ls = x32_sum(Ls);
    const float wn = wt / Ls;
    const float w0 = dpp<0x00>(wn), w1 = dpp<0x55>(wn), w2 = dpp<0xAA>(wn), w3 = dpp<0xFF>(wn);
    float o[8];
#pragma unroll
    for (int i = 0; i < 4; i++) {
      o[2 * i] = (w0 * bflo(od[0][i]) + w1 * bflo(od[1][i])) + (w2 * bflo(od[2][i]) + w3 * bflo(od[3][i]));
      o[2 * i + 1] = (w0 * bfhi(od[0][i]) + w1 * bfhi(od[1][i])) + (w2 * bfhi(od[2][i]) + w3 * bfhi(od[3][i]));
    }
    float z[4];
#pragma unroll
    for (int i = 0; i < 4; i++) {
      auto s = __builtin_amdgcn_permlane32_swap(f2u(o[i]), f2u(o[i + 4]), false, false);
      z[i] = u2f(s[0]) + u2f(s[1]);
    }
    u16* sc = &L.oscr[w * 256];
    *(u32x2*)&sc[b * 32 + ch * 8 + sh * 4] = u32x2{pack2(z[0], z[1]), pack2(z[2], z[3])};
    asm volatile("s_waitcnt lgkmcnt(0)" ::: "memory");
    ofrag = *(const u32x4*)&sc[(lane & 7) * 32 + g * 8];
  }
  {
    f32x4 acc[8];
#pragma unroll
    for (int t = 0; t < 8; t++) acc[t] = mfma16(ofrag, Bo[t], f32x4{0.f, 0.f, 0.f, 0.f});
    w3_store(acc, s3 + 8 * S3_STRIDE, lane);
  }
  lds_signal(&L.ctrl[C_O3], lane);
  lds_wait(&L.ctrl[C_O3], 4);
  {
    const int sub = (lane >> 3) + 8 * w, b = lane & 7;
    st_sc1(rws, WS_FP + (u32)(((ct_o * 32 + sub) * 64 + h_o) * 128 + b * 16), enc(w3_sum(L.kimg, 1, sub, b), T));
  }
  if (w == 0) STAMP(16);
}

extern "C" void kernel_launch(void* const* d_in, const int* in_sizes, int n_in,
                              void* d_out, int out_size, void* d_ws, size_t ws_size,
                              hipStream_t stream) {
  fused_decode_kernel<<<256, 768, 0, stream>>>((const u16*)d_in[0], (const u16*)d_in[1], (const u16*)d_in[2],
                                               (const u16*)d_in[3], (const u16*)d_in[4], (const u16*)d_in[6],
                                               (const u16*)d_in[7], (const u16*)d_in[8], (u16*)d_out,
                                               (unsigned char*)d_ws);
}
```

```cpp
#include <hip/hip_runtime.h>
#include <stdint.h>

typedef unsigned int u32;
typedef unsigned long long u64;
typedef unsigned short u16;
typedef __attribute__((ext_vector_type(4))) unsigned int u32x4;
typedef __attribute__((ext_vector_type(2))) unsigned int u32x2;
typedef __attribute__((ext_vector_type(4))) float f32x4;
typedef __attribute__((ext_vector_type(8))) __bf16 bf16x8;
typedef __attribute__((ext_vector_type(2))) __bf16 bf16x2;
typedef __amdgpu_buffer_rsrc_t rsrc_t;

#define STAMPS 0

constexpr u32 KV_BYTES = 8u * 4u * 4096u * 128u * 2u;
constexpr u32 WQ_BYTES = 1024u * 4096u * 2u;
constexpr u32 WG_BYTES = 1024u * 3584u * 2u;

constexpr u32 WS_EPOCH = 0;
constexpr u32 WS_QP = 4096;
constexpr u32 WS_GP = WS_QP + 524288u;
constexpr u32 WS_OP = WS_GP + 112u * 4096u;
constexpr u32 WS_ST = WS_OP + 524288u;
constexpr u32 WS_FP = WS_ST + 32768u;
constexpr u32 WS_END = WS_FP + 256u * 8192u;

constexpr int AUX_NT = 2, AUX_SC1 = 16;
constexpr u32 SENT = 0xFFFFFFFFu;
constexpr u32 TMASK = 0x3FFu;
constexpr int LDS_SPIN_CAP = 1 << 21;
constexpr int GLB_SPIN_CAP = 1 << 17;

enum { C_LN = 0, C_CL, C_P1Q, C_P1G, C_P1I, C_X, C_Q, C_KI, C_M, C_P, C_SC, C_VI, C_RD, C_RO, C_OF, C_D3, C_O3, C_G, C_OPUB, C_N };

#define CBAR() asm volatile("" ::: "memory")
#define WIN 8
#define WINV 8
#define WIN1A 4
#define WIN1 WIN
#define WIN3 WIN
#define PSLEEP 4
template <int N> __device__ __forceinline__ void wait_vm() { asm volatile("s_waitcnt vmcnt(%0)" ::"n"(N) : "memory"); }

__device__ __forceinline__ rsrc_t mk_rsrc(const void* p, u32 bytes) {
  return __builtin_amdgcn_make_buffer_rsrc((void*)p, (short)0, (int)bytes, 0x00020000);
}
__device__ __forceinline__ u32x4 ld_nt(rsrc_t r, u32 voff, u32 soff) { return __builtin_amdgcn_raw_buffer_load_b128(r, (int)voff, (int)soff, AUX_NT); }
__device__ __forceinline__ u32x4 ld_sc1(rsrc_t r, u32 voff, u32 soff) { return __builtin_amdgcn_raw_buffer_load_b128(r, (int)voff, (int)soff, AUX_SC1); }
__device__ __forceinline__ void st_sc1(rsrc_t r, u32 voff, u32x4 v) { __builtin_amdgcn_raw_buffer_store_b128(v, r, (int)voff, 0, AUX_SC1); }
__device__ __forceinline__ void dma16(u32x4 srd, u32 voff, u32 soff, u32 ldsaddr) {
  u32 keep;
  asm volatile("s_mov_b32 %0, m0\n\ts_mov_b32 m0, %4\n\ts_nop 0\n\tbuffer_load_dwordx4 %1, %2, %3 offen nt lds\n\ts_mov_b32 m0, %0"
               : "=&s"(keep) : "v"(voff), "s"(srd), "s"(soff), "s"(ldsaddr) : "memory");
}
__device__ __forceinline__ u32x4 mk_srd(const void* p, u32 bytes) {
  const u64 a = (u64)p;
  return u32x4{(u32)a, (u32)(a >> 32) & 0xFFFFu, bytes, 0x00020000u};
}
typedef short v4i16_t __attribute__((ext_vector_type(4)));
typedef __attribute__((address_space(3))) unsigned char* lds_ptr;
__device__ __forceinline__ u32x4 tr_frag(lds_ptr p0, lds_ptr p1) {
  const u32x2 a = __builtin_bit_cast(u32x2, __builtin_amdgcn_ds_read_tr16_b64_v4i16((__attribute__((address_space(3))) v4i16_t*)p0));
  const u32x2 b = __builtin_bit_cast(u32x2, __builtin_amdgcn_ds_read_tr16_b64_v4i16((__attribute__((address_space(3))) v4i16_t*)p1));
  return u32x4{a[0], a[1], b[0], b[1]};
}

__device__ __forceinline__ float u2f(u32 x) { return __uint_as_float(x); }
__device__ __forceinline__ u32 f2u(float x) { return __float_as_uint(x); }
__device__ __forceinline__ float bflo(u32 x) { return u2f(x << 16); }
__device__ __forceinline__ float bfhi(u32 x) { return u2f(x & 0xFFFF0000u); }
__device__ __forceinline__ u32 pack2(float lo, float hi) {
  bf16x2 v = {(__bf16)lo, (__bf16)hi};
  return __builtin_bit_cast(u32, v);
}
__device__ __forceinline__ float rbf(float x) { return (float)(__bf16)x; }
template <int CTRL> __device__ __forceinline__ float dpp(float x) {
  return __builtin_bit_cast(float, __builtin_amdgcn_mov_dpp(__builtin_bit_cast(int, x), CTRL, 0xf, 0xf, true));
}
constexpr int XOR1 = 0xB1, XOR2 = 0x4E, XOR7 = 0x141, XOR8 = 0x128;
__device__ __forceinline__ float x32_max(float x) {
  auto t = __builtin_amdgcn_permlane32_swap(f2u(x), f2u(x), false, false);
  return fmaxf(u2f(t[0]), u2f(t[1]));
}
__device__ __forceinline__ float x32_sum(float x) {
  auto t = __builtin_amdgcn_permlane32_swap(f2u(x), f2u(x), false, false);
  return u2f(t[0]) + u2f(t[1]);
}
__device__ __forceinline__ float xrow_max(float x) {
  auto s = __builtin_amdgcn_permlane16_swap(f2u(x), f2u(x), false, false);
  return x32_max(fmaxf(u2f(s[0]), u2f(s[1])));
}
__device__ __forceinline__ float xrow_sum(float x) {
  auto s = __builtin_amdgcn_permlane16_swap(f2u(x), f2u(x), false, false);
  return x32_sum(u2f(s[0]) + u2f(s[1]));
}
__device__ __forceinline__ float wave_sum(float x) {
  x += dpp<XOR1>(x); x += dpp<XOR2>(x); x += dpp<XOR7>(x); x += dpp<XOR8>(x);
  return xrow_sum(x);
}
__device__ __forceinline__ float half_sum(float x) {
  x += dpp<XOR1>(x); x += dpp<XOR2>(x); x += dpp<XOR7>(x); x += dpp<XOR8>(x);
  auto s = __builtin_amdgcn_permlane16_swap(f2u(x), f2u(x), false, false);
  return u2f(s[0]) + u2f(s[1]);
}
__device__ __forceinline__ float fdiv(float a, float d, float inv) {
  const float q = a * inv;
  return __builtin_fmaf(__builtin_fmaf(-q, d, a), inv, q);
}
__device__ __forceinline__ f32x4 mfma16(u32x4 a, u32x4 b, f32x4 c) {
  return __builtin_amdgcn_mfma_f32_16x16x32_bf16(__builtin_bit_cast(bf16x8, a), __builtin_bit_cast(bf16x8, b), c, 0, 0, 0);
}
__device__ __forceinline__ u32x4 tcol(const u32x4* R, int t) {
  const u32 sel = (t & 1) ? 0x07060302u : 0x05040100u;
  const int d = t >> 1;
  u32x4 r;
  r[0] = __builtin_amdgcn_perm(R[1][d], R[0][d], sel);
  r[1] = __builtin_amdgcn_perm(R[3][d], R[2][d], sel);
  r[2] = __builtin_amdgcn_perm(R[5][d], R[4][d], sel);
  r[3] = __builtin_amdgcn_perm(R[7][d], R[6][d], sel);
  return r;
}
__device__ __forceinline__ u32 umax3(u32 a, u32 b, u32 c) { return max(max(a, b), c); }
__device__ __forceinline__ u32 umax4(u32x4 v) { return max(umax3(v[0], v[1], v[2]), v[3]); }
__device__ __forceinline__ u32x4 canon(u32x4 v) {
  v[0] = min(v[0], 0xFFFFFFFEu); v[1] = min(v[1], 0xFFFFFFFEu); v[2] = min(v[2], 0xFFFFFFFEu); v[3] = min(v[3], 0xFFFFFFFEu);
  return v;
}
__device__ __forceinline__ u32x4 enc(f32x4 v, u32x4 T) {
  u32x4 r;
#pragma unroll
  for (int i = 0; i < 4; i++) {
    u32 u = f2u(v[i]);
    u = ((u & 0x7FFFFFFFu) >= 0x7F800000u) ? u : u + 0x200u;
    r[i] = (u & ~TMASK) | (T[i] & TMASK);
  }
  return r;
}
__device__ __forceinline__ f32x4 dec(u32x4 w) {
  return f32x4{u2f(w[0] & ~TMASK), u2f(w[1] & ~TMASK), u2f(w[2] & ~TMASK), u2f(w[3] & ~TMASK)};
}
__device__ __forceinline__ u32 tdiff(u32x4 w, u32x4 T) { return ((w[0] ^ T[0]) | (w[1] ^ T[1])) | ((w[2] ^ T[2]) | (w[3] ^ T[3])); }

template <int SL = 1> __device__ __forceinline__ void lds_wait(u32* p, u32 target) {
  CBAR();
#pragma nounroll
  for (int i = 0; i < LDS_SPIN_CAP; i++) {
    u32 v = __hip_atomic_load(p, __ATOMIC_RELAXED, __HIP_MEMORY_SCOPE_WORKGROUP);
    if (__builtin_amdgcn_readfirstlane(v) >= target) break;
    __builtin_amdgcn_s_sleep(SL);
  }
  CBAR();
}
__device__ __forceinline__ u32 lds_arrive(u32* p, int lane) {
  asm volatile("s_waitcnt lgkmcnt(0)" ::: "memory");
  u32 old = 0;
  if (lane == 0) old = __hip_atomic_fetch_add(p, 1u, __ATOMIC_RELAXED, __HIP_MEMORY_SCOPE_WORKGROUP);
  old = __builtin_amdgcn_readfirstlane(old);
  CBAR();
  return old;
}
__device__ __forceinline__ void lds_signal(u32* p, int lane) {
  asm volatile("s_waitcnt lgkmcnt(0)" ::: "memory");
  if (lane == 0) __hip_atomic_fetch_add(p, 1u, __ATOMIC_RELAXED, __HIP_MEMORY_SCOPE_WORKGROUP);
  CBAR();
}

constexpr int XN_STRIDE = 1024 + 8;
constexpr int SL_STRIDE = 68;
constexpr int Q_STRIDE = 136;
constexpr int OV_XN = 65536, OV_SQ = OV_XN + 8 * XN_STRIDE * 2, OV_SG = OV_SQ + 4 * 8 * SL_STRIDE * 4;
static_assert(OV_SG + 4 * 8 * SL_STRIDE * 4 <= 131072, "overlay");
constexpr int OFF_RO = 0, OFF_RD = 8192;

struct __align__(16) Lds {
  unsigned char img[131072];
  u32x4 P[16 * 4 * 8];
  u32x4 ofr[4 * 64];
  u32x4 hfr[4 * 64];
  u16 qs[8 * Q_STRIDE];
  float wm[64];
  float wl[64];
  float scale[8];
  float gate[32];
  u32 ctrl[24];
  u32 vprog[4];
  u32 pvprog[4];
  u32 tag[4];
};

__device__ __forceinline__ void w_epilogue(const f32x4* acc, float* slab, int lane) {
  float val[2][8];
#pragma unroll
  for (int t = 0; t < 8; t++) {
#pragma unroll
    for (int r = 0; r < 2; r++) {
      auto s = __builtin_amdgcn_permlane32_swap(f2u(acc[t][r]), f2u(acc[t][r + 2]), false, false);
      val[r][t] = u2f(s[0]) + dpp<XOR8>(u2f(s[1]));
    }
  }
  if (!(lane & 8)) {
    const int b0 = 4 * ((lane >> 4) & 1) + 2 * (lane >> 5);
    const int cp = lane & 7;
#pragma unroll
    for (int r = 0; r < 2; r++) {
      float* p = slab + (b0 + r) * SL_STRIDE + 8 * cp;
      *(f32x4*)p = f32x4{val[r][0], val[r][1], val[r][2], val[r][3]};
      *(f32x4*)(p + 4) = f32x4{val[r][4], val[r][5], val[r][6], val[r][7]};
    }
  }
}
__device__ __forceinline__ f32x4 slab_sum(const float* slab, int b, int col) {
  const float* p = slab + b * SL_STRIDE + col;
  f32x4 a = *(const f32x4*)p;
  f32x4 bb = *(const f32x4*)(p + 8 * SL_STRIDE);
  f32x4 c = *(const f32x4*)(p + 16 * SL_STRIDE);
  f32x4 d = *(const f32x4*)(p + 24 * SL_STRIDE);
  return (a + bb) + (c + d);
}
__device__ __forceinline__ u32x4 read_tag(const u32* p) {
  u32x4 t = *(const u32x4*)p;
  t[0] = __builtin_amdgcn_readfirstlane(t[0]); t[1] = __builtin_amdgcn_readfirstlane(t[1]);
  t[2] = __builtin_amdgcn_readfirstlane(t[2]); t[3] = __builtin_amdgcn_readfirstlane(t[3]);
  return t;
}
__device__ __forceinline__ u32 lds_addr(const void* p) { return (u32)(uintptr_t)(lds_ptr)p; }
__device__ __forceinline__ u32 prog_min(const u32* p) {
  u32x4 t;
  asm volatile("ds_read_b128 %0, %1\n\ts_waitcnt lgkmcnt(0)" : "=v"(t) : "v"(lds_addr(p)) : "memory");
  return __builtin_amdgcn_readfirstlane(min(min(t[0], t[1]), min(t[2], t[3])));
}
__device__ __forceinline__ void prog_wait(const u32* p, u32 target) {
#pragma nounroll
  for (int i = 0; i < LDS_SPIN_CAP; i++) {
    if (prog_min(p) >= target) break;
    __builtin_amdgcn_s_sleep(1);
  }
}
__device__ __forceinline__ void prog_set(u32* p, u32 val, int lane) {
  CBAR();
  if (lane == 0) __hip_atomic_store(p, val, __ATOMIC_RELAXED, __HIP_MEMORY_SCOPE_WORKGROUP);
  CBAR();
}
__device__ __forceinline__ void tile_voff(int lane, u32* vo) {
  const int r = lane >> 4, pos = (lane & 15) >> 1;
#pragma unroll
  for (int i = 0; i < 2; i++) vo[i] = (u32)(r * 2048 + ((pos ^ (4 * i + r)) * 32) + (lane & 1) * 16);
}
__device__ __forceinline__ void tile_frags(lds_ptr img, int off, int w, int lane, u32x4 (*F)[2]) {
  const int g = lane >> 4, q = (lane >> 2) & 3, p = lane & 3;
#pragma unroll
  for (int t = 0; t < 2; t++) {
    lds_ptr b = img + off + (4 * g + q) * 256 + (((2 * w + t) ^ (4 * (g & 1) + q)) * 32) + p * 8;
#pragma unroll
    for (int kq = 0; kq < 4; kq++) F[kq][t] = tr_frag(b + kq * 32768, b + kq * 32768 + 4096);
  }
}

#define STAMP(i) do { } while (0)

__global__ void __launch_bounds__(832, 4)
fused_decode_kernel(const u16* x, const u16* kc, const u16* vc,
                    const u16* wq, const u16* wo, const u16* wg,
                    const u16* wu, const u16* wd, u16* out,
                    unsigned char* ws) {
  __shared__ Lds L;
  const int tid = threadIdx.x;
  const int lane = tid & 63;
  const int wave = __builtin_amdgcn_readfirstlane(tid >> 6);
  const int c = blockIdx.x;
  const int hkv = c >> 6, gi = c & 63;
  const int ab = gi >> 3, as = gi & 7;
  const bool mlp = c < 224;
  const rsrc_t rws = mk_rsrc(ws, WS_END);
  u16* xn = (u16*)(L.img + OV_XN);
  const int g = lane >> 4;
  const lds_ptr img = (lds_ptr)L.img;
  const u32 img_addr = (u32)(uintptr_t)img;
  const u32 kvbase = (u32)((ab * 4 + hkv) * 4096 + as * 512) * 256u;
  const f32x4 zero4 = {0.f, 0.f, 0.f, 0.f};

  if (wave == 12) {
    const u32x4 ep = ld_sc1(rws, WS_EPOCH, 0);
    st_sc1(rws, WS_OP + (u32)(c * 2048 + lane * 16), u32x4{SENT, SENT, SENT, SENT});
    st_sc1(rws, WS_OP + (u32)(c * 2048 + 1024 + lane * 16), u32x4{SENT, SENT, SENT, SENT});
    if (lane < 32) L.ctrl[lane] = 0;
    asm volatile("s_waitcnt lgkmcnt(0)" ::: "memory");
    __builtin_amdgcn_sched_barrier(0);
    CBAR();
    __builtin_amdgcn_s_barrier();
    CBAR();
    u32x4 T;
    {
      u64 ea = ((u64)ep[1] << 32) | ep[0], eb = ((u64)ep[3] << 32) | ep[2];
      u64 h1 = (ea ^ (eb * 0x9E3779B97F4A7C15ull)) * 6364136223846793005ull + 1442695040888963407ull;
      h1 ^= h1 >> 29; h1 *= 0xBF58476D1CE4E5B9ull; h1 ^= h1 >> 32;
      u64 h2 = (h1 + eb) * 0x94D049BB133111EBull + 0x2545F4914F6CDD1Dull;
      h2 ^= h2 >> 31;
      u32x4 H = {(u32)h1, (u32)(h1 >> 32), (u32)h2, (u32)(h2 >> 32)};
#pragma unroll
      for (int i = 0; i < 4; i++) T[i] = (H[i] & ~TMASK) | ((ep[i] + 1u + (H[i] & 0x1FFu)) & TMASK);
      if (lane == 0) *(u32x4*)L.tag = T;
    }
    lds_signal(&L.ctrl[C_CL], lane);
    __builtin_amdgcn_s_setprio(2);
    {
      const int ct_q = hkv * 16 + (gi >> 2), ks_q = gi & 3;
      const float* sq = (const float*)(L.img + OV_SQ);
      lds_wait(&L.ctrl[C_P1Q], 4);
      const f32x4 s0 = slab_sum(sq, g, 4 * (lane & 15)), s1 = slab_sum(sq, g + 4, 4 * (lane & 15));
      const u32x4 e0 = enc(s0, T), e1 = enc(s1, T);
      const u32 qo = WS_QP + (u32)((g * 4 + ks_q) * 4096 + ct_q * 64 + 4 * (lane & 15)) * 4u;
      asm volatile("s_waitcnt vmcnt(0)" ::: "memory");
      STAMP(1);
      st_sc1(rws, qo, e0);
      st_sc1(rws, qo + 4u * 4u * 4096u * 4u, e1);
      STAMP(7);
    }
    {
      const int mat = c >= 112 ? 1 : 0;
      const int mm = c - 112 * mat;
      const int ct_g = mm >> 1, ks_g = mm & 1;
      const float* sg = (const float*)(L.img + OV_SG);
      lds_wait(&L.ctrl[C_P1G], 4);
      const int b = lane & 7, hg = lane >> 3;
      const f32x4 s0 = slab_sum(sg, b, 4 * hg), s1 = slab_sum(sg, b, 32 + 4 * hg);
      lds_signal(&L.ctrl[C_X], lane);
      const u32 go = WS_GP + (u32)(((((ct_g * 2) * 2 + ks_g) * 8 + hg) * 2 + mat) * 128 + b * 16);
      if (mlp) {
        st_sc1(rws, go, enc(s0, T));
        st_sc1(rws, go + 4096u, enc(s1, T));
      }
      STAMP(10);
    }
    {
      lds_wait<2>(&L.ctrl[C_P], 8);
      const f32x4 m0 = *(const f32x4*)&L.wm[(lane & 7) * 8], m1 = *(const f32x4*)&L.wm[(lane & 7) * 8 + 4];
      const float Mh = fmaxf(fmaxf(fmaxf(m0[0], m0[1]), fmaxf(m0[2], m0[3])), fmaxf(fmaxf(m1[0], m1[1]), fmaxf(m1[2], m1[3])));
      const f32x4 l0 = *(const f32x4*)&L.wl[(lane & 7) * 8], l1 = *(const f32x4*)&L.wl[(lane & 7) * 8 + 4];
      const float Ls = ((l0[0] + l0[1]) + (l0[2] + l0[3])) + ((l1[0] + l1[1]) + (l1[2] + l1[3]));
      if (lane < 8) st_sc1(rws, WS_ST + (u32)((((hkv * 8 + lane) * 8 + ab) * 8 + as) * 16), u32x4{f2u(Mh), f2u(Ls), T[2], T[3]});
      STAMP(3);
    }
    return;
  }
  if (wave >= 4) {
    const int v = wave - 4;
    const bool grpB = v >= 4;
    const int a = v & 3;
    if (v == 0) STAMP(0);
    const rsrc_t rx = mk_rsrc(x, 8u * 1024u * 2u);
    u32x2 xfin = {0u, 0u};
    const u32 obox = WS_OP + (u32)((((((hkv * 8 + (lane & 7)) * 4 + a) * 8 + as) * 8 + ab) * 4 + g) * 16);
    if (grpB) {
      const int ct_q = hkv * 16 + (gi >> 2), ks_q = gi & 3;
      const int row = a + 4 * (lane >> 5);
      u32x4 xr[4];
#pragma unroll
      for (int i = 0; i < 4; i++) xr[i] = __builtin_amdgcn_raw_buffer_load_b128(rx, row * 2048 + (lane & 31) * 16 + i * 512, 0, 0);
      u32x4 Rq[8];
      const rsrc_t rq = mk_rsrc(wq, WQ_BYTES);
      const u32 qoff = (u32)(16 * g + 8 * ((lane >> 3) & 1)) * 8192u + (lane & 7) * 16;
      const u32 qbase = (u32)(ks_q * 256 + a * 64) * 8192u + ct_q * 128;
#pragma unroll
      for (int i = 0; i < 8; i++) Rq[i] = ld_nt(rq, qoff, qbase + i * 8192);
      __builtin_amdgcn_sched_barrier(0);
      CBAR();
      __builtin_amdgcn_s_barrier();
      CBAR();
      __builtin_amdgcn_s_setprio(2);
      {
        float e[32];
#pragma unroll
        for (int i = 0; i < 4; i++)
#pragma unroll
          for (int j = 0; j < 4; j++) { e[8 * i + 2 * j] = bflo(xr[i][j]); e[8 * i + 2 * j + 1] = bfhi(xr[i][j]); }
        float s = 0.f;
#pragma unroll
        for (int i = 0; i < 32; i++) s += e[i];
        const float mean = half_sum(s) * (1.f / 1024.f);
        float s2 = 0.f;
#pragma unroll
        for (int i = 0; i < 32; i++) { float d = e[i] - mean; s2 += d * d; }
        const float var = half_sum(s2) * (1.f / 1024.f);
        const float mb = rbf(mean), vb = rbf(var);
        const float den = rbf(sqrtf(rbf(vb + 1e-5f)));
        const float inv = 1.f / den;
#pragma unroll
        for (int i = 0; i < 4; i++) {
          u32x4 y;
#pragma unroll
          for (int j = 0; j < 4; j++) {
            const u32 d2 = pack2(e[8 * i + 2 * j] - mb, e[8 * i + 2 * j + 1] - mb);
            y[j] = pack2(fdiv(bflo(d2), den, inv), fdiv(bfhi(d2), den, inv));
          }
          *(u32x4*)&xn[row * XN_STRIDE + (lane & 31) * 8 + i * 256] = y;
        }
      }
      lds_signal(&L.ctrl[C_LN], lane);
      lds_wait(&L.ctrl[C_LN], 4);
      const int r = lane & 15;
      const u32x4 af = *(const u32x4*)&xn[(r & 7) * XN_STRIDE + 16 * g + 8 * (r >> 3) + ks_q * 256 + a * 64];
      f32x4 acc[8];
#pragma unroll
      for (int t = 0; t < 8; t++) acc[t] = mfma16(af, tcol(Rq, t), zero4);
      float* sq = (float*)(L.img + OV_SQ);
      w_epilogue(acc, sq + a * 8 * SL_STRIDE, lane);
      lds_signal(&L.ctrl[C_P1Q], lane);
      __builtin_amdgcn_s_setprio(0);
      lds_wait(&L.ctrl[C_KI], 4);
    } else {
      if (v == 0) xfin = __builtin_amdgcn_raw_buffer_load_b64(rx, (lane & 7) * 2048 + c * 8, 0, 0);
      __builtin_amdgcn_sched_barrier(0);
      CBAR();
      __builtin_amdgcn_s_barrier();
      CBAR();
      lds_wait(&L.ctrl[C_P1I], 4);
    }
    u32x4 K[16];
    {
      const rsrc_t rk = mk_rsrc(kc, KV_BYTES);
      const u32 koff = (u32)(((lane >> 2) & 3) * 2048 + g * 64 + (lane & 3) * 16);
#pragma unroll
      for (int n = 0; n < 16; n++) {
        if (n >= WIN) wait_vm<WIN - 1>();
        K[n] = ld_nt(rk, koff, kvbase + v * 16384 + ((n & 1) * 32 + (n >> 1)) * 256);
      }
    }
    __builtin_amdgcn_sched_barrier(0);
    lds_signal(&L.ctrl[C_KI], lane);
    if (v == 3) STAMP(19);
    if (v == 7) STAMP(2);
    lds_wait(&L.ctrl[C_CL], 1);
    const u32x4 T = read_tag(L.tag);
    if (!grpB) {
      wait_vm<0>();
      const u32 qo = WS_QP + (u32)(ab * 4 * 4096 + hkv * 1024 + a * 256 + 4 * lane) * 4u;
      u32x4 q[4];
      bool need[4] = {true, true, true, true};
#pragma nounroll
      for (int it = 0; it < GLB_SPIN_CAP; it++) {
        CBAR();
#pragma unroll
        for (int i = 0; i < 4; i++)
          if (need[i]) q[i] = ld_sc1(rws, qo, i * 16384);
#pragma unroll
        for (int i = 0; i < 4; i++) need[i] = (tdiff(q[i], T) & TMASK) != 0;
        if (!__any(need[0] | need[1] | need[2] | need[3])) break;
        __builtin_amdgcn_s_sleep(PSLEEP);
      }
      CBAR();
      const f32x4 qsum = (dec(q[0]) + dec(q[1])) + (dec(q[2]) + dec(q[3]));
      *(u32x2*)&L.qs[(2 * a + (lane >> 5)) * Q_STRIDE + ((4 * lane) & 127)] = u32x2{pack2(qsum[0], qsum[1]), pack2(qsum[2], qsum[3])};
      lds_signal(&L.ctrl[C_Q], lane);
      if (v == 0) STAMP(12);
    }
    const int kb_d = c >> 3, ct_d = c & 7;
    const u32 ho = WS_GP + (u32)(kb_d * 4 + a) * 4096u + lane * 16;
    u32x4 hp[4];
    lds_wait(&L.ctrl[C_Q], 4);
    __builtin_amdgcn_s_setprio(1);
    const bool hi = (lane >> 3) & 1;
    float sc[8];
    float Mh;
    {
      const u16* qp = &L.qs[(lane & 7) * Q_STRIDE + 32 * g + 8 * ((lane >> 3) & 1)];
      const u32x4 q0 = *(const u32x4*)qp, q1 = *(const u32x4*)(qp + 16);
      float m = -INFINITY;
#pragma unroll
      for (int mm = 0; mm < 8; mm++) {
        const f32x4 d0 = mfma16(K[2 * mm], q0, zero4), d1 = mfma16(K[2 * mm], q1, zero4);
        const f32x4 e0 = mfma16(K[2 * mm + 1], q0, zero4), e1 = mfma16(K[2 * mm + 1], q1, zero4);
        const float r0 = (d0[0] + d1[2]) + dpp<XOR8>(d0[1] + d1[3]);
        const float r1 = (e0[1] + e1[3]) + dpp<XOR8>(e0[0] + e1[2]);
        const float z = rbf(rbf(hi ? r1 : r0) * 0.08838834764831845f);
        sc[mm] = z;
        m = fmaxf(m, z);
      }
      m = fmaxf(m, dpp<XOR8>(m));
      m = xrow_max(m);
      if (lane < 8) L.wm[lane * 8 + v] = m;
      lds_signal(&L.ctrl[C_M], lane);
      lds_wait(&L.ctrl[C_M], 8);
      const f32x4 m0 = *(const f32x4*)&L.wm[(lane & 7) * 8], m1 = *(const f32x4*)&L.wm[(lane & 7) * 8 + 4];
      Mh = fmaxf(fmaxf(fmaxf(m0[0], m0[1]), fmaxf(m0[2], m0[3])), fmaxf(fmaxf(m1[0], m1[1]), fmaxf(m1[2], m1[3])));
      float ls = 0.f;
#pragma unroll
      for (int i = 0; i < 8; i++) { sc[i] = __expf(sc[i] - Mh); ls += sc[i]; }
      L.P[((2 * v + (hi ? 1 : 0)) * 4 + g) * 8 + (lane & 7)] = u32x4{pack2(sc[0], sc[1]), pack2(sc[2], sc[3]), pack2(sc[4], sc[5]), pack2(sc[6], sc[7])};
      ls += dpp<XOR8>(ls);
      ls = xrow_sum(ls);
      if (lane < 8) L.wl[lane * 8 + v] = ls;
      lds_signal(&L.ctrl[C_P], lane);
    }
    if (!grpB) {
      lds_wait(&L.ctrl[C_P], 8);
      __builtin_amdgcn_s_setprio(2);
      f32x4 acc0 = zero4, acc1 = zero4;
      {
        const int q = (lane >> 2) & 3, p = lane & 3;
        const lds_ptr b0 = img + g * 32768 + q * 256 + (((2 * a) ^ (2 * q + (g & 1))) * 32) + p * 8;
        const lds_ptr b1 = img + g * 32768 + q * 256 + (((2 * a + 1) ^ (2 * q + (g & 1))) * 32) + p * 8;
        const u32x4* pp = &L.P[g * 8 + (lane & 7)];
        u32 j = 0;
#pragma nounroll
        for (int it = 0; it < LDS_SPIN_CAP; it++) {
          const u32 avail = prog_min(L.vprog);
          if (avail <= j) { __builtin_amdgcn_s_sleep(1); continue; }
#pragma nounroll
          for (; j < avail; j++) {
            const u32x4 pf = pp[j * 32];
            const u32x4 f0 = tr_frag(b0 + j * 2048, b0 + j * 2048 + 1024);
            const u32x4 f1 = tr_frag(b1 + j * 2048, b1 + j * 2048 + 1024);
            acc0 = mfma16(f0, pf, acc0);
            acc1 = mfma16(f1, pf, acc1);
          }
          if (j >= 16) break;
          asm volatile("s_waitcnt lgkmcnt(0)" ::: "memory");
          prog_set(&L.pvprog[a], j, lane);
        }
      }
      if (v == 0) STAMP(4);
      {
        u32x4 ov = {pack2(acc0[0], acc0[1]), pack2(acc0[2], acc0[3]), pack2(acc1[0], acc1[1]), pack2(acc1[2], acc1[3])};
        ov = canon(ov);
        if (!(lane & 8)) st_sc1(rws, obox, ov);
      }
      prog_set(&L.pvprog[a], 16, lane);
      lds_signal(&L.ctrl[C_OPUB], lane);
      if (v == 0) STAMP(5);
      if (v >= 2) return;
      if (v == 0) lds_wait(&L.ctrl[C_O3], 4);
      else if (mlp) lds_wait<4>(&L.ctrl[C_D3], 4);
      const u32 fo = WS_FP + (u32)c * 8192u + v * 4096 + lane * 16;
      u32x4 f[4];
      f[3] = u32x4{T[0] & TMASK, T[1] & TMASK, T[2] & TMASK, T[3] & TMASK};
      bool need[4] = {true, true, true, !(v == 1 && lane >= 32)};
#pragma nounroll
      for (int it = 0; it < GLB_SPIN_CAP; it++) {
        CBAR();
#pragma unroll
        for (int i = 0; i < 4; i++)
          if (need[i]) f[i] = ld_sc1(rws, fo + i * 1024, 0);
#pragma unroll
        for (int i = 0; i < 4; i++) need[i] = (tdiff(f[i], T) & TMASK) != 0;
        if (!__any(need[0] | need[1] | need[2] | need[3])) break;
        __builtin_amdgcn_s_sleep(1);
      }
      CBAR();
      if (v == 0) STAMP(17);
      f32x4 S = (dec(f[0]) + dec(f[1])) + (dec(f[2]) + dec(f[3]));
#pragma unroll
      for (int i = 0; i < 4; i++) { float s = S[i]; s += dpp<XOR8>(s); S[i] = rbf(xrow_sum(s)); }
      if (v == 1) {
        f32x4 G;
#pragma unroll
        for (int i = 0; i < 4; i++) G[i] = rbf(S[i] / (1.f + __expf(-S[i])));
        if (lane < 8) *(f32x4*)&L.gate[lane * 4] = G;
        lds_signal(&L.ctrl[C_G], lane);
        STAMP(14);
        return;
      }
      lds_wait(&L.ctrl[C_G], 1);
      const f32x4 G = *(const f32x4*)&L.gate[(lane & 7) * 4];
      float r[4];
#pragma unroll
      for (int i = 0; i < 4; i++) {
        const float xv = (i & 1) ? bfhi(xfin[i >> 1]) : bflo(xfin[i >> 1]);
        r[i] = xv + rbf(S[i] * G[i]);
      }
      if (lane < 8) *(u32x2*)&out[lane * 1024 + c * 4] = u32x2{pack2(r[0], r[1]), pack2(r[2], r[3])};
      if (c == 0 && lane == 0) st_sc1(rws, WS_EPOCH, T);
      STAMP(18);
      return;
    }
    __builtin_amdgcn_s_setprio(0);
    if (mlp) {
      u32x4* p = hp;
      bool need[4] = {true, true, true, true};
#pragma nounroll
      for (int it = 0; it < GLB_SPIN_CAP; it++) {
        if (!__any(need[0] | need[1] | need[2] | need[3])) break;
        CBAR();
#pragma unroll
        for (int i = 0; i < 4; i++)
          if (need[i]) p[i] = ld_sc1(rws, ho + i * 1024, 0);
#pragma unroll
        for (int i = 0; i < 4; i++) need[i] = (tdiff(p[i], T) & TMASK) != 0;
        if (it) __builtin_amdgcn_s_sleep(PSLEEP);
      }
      CBAR();
      float hv[8];
#pragma unroll
      for (int hf = 0; hf < 2; hf++) {
        const f32x4 S = dec(p[hf]) + dec(p[2 + hf]);
#pragma unroll
        for (int i = 0; i < 4; i++) {
          const float own = rbf(S[i]);
          const float oth = dpp<XOR8>(own);
          const float gg = hi ? oth : own, uu = hi ? own : oth;
          const float sg = rbf(1.f / (1.f + __expf(-gg)));
          hv[hf * 4 + i] = sg * uu;
        }
      }
      L.hfr[a * 64 + lane] = u32x4{pack2(hv[0], hv[1]), pack2(hv[2], hv[3]), pack2(hv[4], hv[5]), pack2(hv[6], hv[7])};
    }
    if (v == 4) STAMP(11);
    if (!mlp) { if (v == 4) { STAMP(8); STAMP(9); } return; }
    lds_wait<2>(&L.ctrl[C_VI], 4);
    prog_wait(L.pvprog, 8);
    {
      u32 vo[2];
      tile_voff(lane, vo);
      const u32x4 sd = mk_srd(wd, WG_BYTES);
      const u32 dbase = (u32)(kb_d * 128 + a * 32) * 2048u + ct_d * 256;
#pragma unroll
      for (int i = 0; i < 8; i++) dma16(sd, vo[i & 1], dbase + i * 8192, img_addr + a * 32768 + OFF_RD + i * 1024);
    }
    wait_vm<0>();
    lds_signal(&L.ctrl[C_RD], lane);
    lds_wait(&L.ctrl[C_RD], 4);
    if (v == 4) STAMP(8);
    {
      u32x4 F[4][2];
      tile_frags(img, OFF_RD, a, lane, F);
      f32x4 acc0 = zero4, acc1 = zero4;
#pragma unroll
      for (int kq = 0; kq < 4; kq++) {
        const u32x4 hf = L.hfr[kq * 64 + lane];
        acc0 = mfma16(F[kq][0], hf, acc0);
        acc1 = mfma16(F[kq][1], hf, acc1);
      }
      const f32x4 r = hi ? acc1 : acc0;
      const int cc = ct_d * 32 + 8 * a + (hi ? 4 : 0) + g;
      st_sc1(rws, WS_FP + (u32)((cc * 64 + 32 + kb_d) * 128 + (lane & 7) * 16), enc(r, T));
    }
    lds_signal(&L.ctrl[C_D3], lane);
    if (v == 4) STAMP(9);
    return;
  }

  const int w = wave;
  const int h_o = hkv * 8 + (gi >> 3), ct_o = gi & 7;
  {
    const int mat = c >= 112 ? 1 : 0;
    const int mm = c - 112 * mat;
    const int ct_g = mm >> 1, ks_g = mm & 1;
    u32x4 Rg[2][8];
    const rsrc_t rg = mk_rsrc(mat ? wu : wg, mlp ? WG_BYTES : 0u);
    const u32 goff = (u32)(16 * g + 8 * ((lane >> 3) & 1)) * 7168u + (lane & 7) * 16;
    const u32 gbase = (u32)(ks_g * 512 + w * 128) * 7168u + ct_g * 128;
#pragma unroll
    for (int n = 0; n < 16; n++) {
      if (n == WIN1A) { __builtin_amdgcn_sched_barrier(0); CBAR(); __builtin_amdgcn_s_barrier(); CBAR(); }
      if (n >= WIN1A && n < 8) wait_vm<WIN1A - 1>();
      if (n >= 8 && n >= WIN1) wait_vm<WIN1 - 1>();
      Rg[n >> 3][n & 7] = ld_nt(rg, goff, gbase + ((n >> 3) * 64 + (n & 7)) * 7168);
    }
    __builtin_amdgcn_sched_barrier(0);
    lds_signal(&L.ctrl[C_P1I], lane);
    lds_wait(&L.ctrl[C_LN], 4);
    const int r = lane & 15;
    const u16* xrow = &xn[(r & 7) * XN_STRIDE + 16 * g + 8 * (r >> 3) + ks_g * 512 + w * 128];
    const u32x4 a0 = *(const u32x4*)xrow;
    const u32x4 a1 = *(const u32x4*)(xrow + 64);
    f32x4 acc[8];
#pragma unroll
    for (int t = 0; t < 8; t++) acc[t] = mfma16(a0, tcol(Rg[0], t), zero4);
#pragma unroll
    for (int t = 0; t < 8; t++) acc[t] = mfma16(a1, tcol(Rg[1], t), acc[t]);
    float* sg = (float*)(L.img + OV_SG);
    w_epilogue(acc, sg + w * 8 * SL_STRIDE, lane);
    lds_signal(&L.ctrl[C_P1G], lane);
  }
  lds_wait<2>(&L.ctrl[C_X], 1);
  const u32x4 T = read_tag(L.tag);
  lds_wait(&L.ctrl[C_KI], 8);
  const u32 sto = WS_ST + (u32)(((h_o * 8 + ((lane >> 2) & 7)) * 8 + 4 * (lane >> 5) + (lane & 3)) * 16);
  u32x4 sd;
  {
    const u32x4 sv = mk_srd(vc, KV_BYTES);
    const int cp = lane & 15;
    const u32 voff = (u32)(g * 256 + (((cp >> 1) ^ (2 * g + (w & 1))) * 32) + (cp & 1) * 16);
    const u32 lb = img_addr + w * 32768;
    u32 vo[2];
    tile_voff(lane, vo);
    const u32x4 so = mk_srd(wo, WQ_BYTES);
    const u32 obase = (u32)(h_o * 128 + w * 32) * 2048u + ct_o * 256;
#pragma unroll
    for (int n = 0; n < 40; n++) {
      if (n >= WINV) {
        wait_vm<WINV - 1>();
        if (((n - WINV + 1) & 1) == 0 && n - WINV + 1 <= 32) prog_set(&L.vprog[w], (n - WINV + 1) >> 1, lane);
      }
      if (n == 32) {
        lds_signal(&L.ctrl[C_VI], lane);
        if (w == 0) STAMP(13);
        prog_wait(L.pvprog, 4);
      }
      if (n < 32) dma16(sv, voff, kvbase + (n >> 1) * 8192 + w * 2048 + (n & 1) * 1024, lb + n * 1024);
      else dma16(so, vo[n & 1], obase + (n - 32) * 8192, lb + OFF_RO + (n - 32) * 1024);
    }
    sd = ld_sc1(rws, sto, 0);
#pragma unroll
    for (int n = 40; n < 32 + WINV; n++) {
      if (((n - WINV + 1) & 1) == 0) {
        asm volatile("s_waitcnt vmcnt(%0)" ::"n"(40 - (n - WINV)) : "memory");
        prog_set(&L.vprog[w], (n - WINV + 1) >> 1, lane);
      }
    }
  }
  wait_vm<0>();
  lds_signal(&L.ctrl[C_RO], lane);
  lds_wait(&L.ctrl[C_RO], 4);
  if (w == 0) STAMP(20);
  u32x4 F[4][2];
  tile_frags(img, OFF_RO, w, lane, F);
#pragma unroll
  for (int kq = 0; kq < 4; kq++) { asm volatile("" : "+v"(F[kq][0])); asm volatile("" : "+v"(F[kq][1])); }
  float w0, w1, w2, w3;
  {
    bool need = ((sd[2] ^ T[2]) | (sd[3] ^ T[3])) != 0;
#pragma nounroll
    for (int it = 0; it < GLB_SPIN_CAP; it++) {
      if (!__any(need)) break;
      CBAR();
      if (need) sd = ld_sc1(rws, sto, 0);
      need = ((sd[2] ^ T[2]) | (sd[3] ^ T[3])) != 0;
      if (it) __builtin_amdgcn_s_sleep(PSLEEP);
    }
    CBAR();
    const float mm = u2f(sd[0]);
    float M = fmaxf(mm, dpp<XOR1>(mm));
    M = fmaxf(M, dpp<XOR2>(M));
    M = x32_max(M);
    const float ew = __expf(mm - M);
    float Ls = ew * u2f(sd[1]);
    Ls += dpp<XOR1>(Ls);
    Ls += dpp<XOR2>(Ls);
    Ls = x32_sum(Ls);
    const float wn = ew / Ls;
    w0 = dpp<0x00>(wn); w1 = dpp<0x55>(wn); w2 = dpp<0xAA>(wn); w3 = dpp<0xFF>(wn);
    if (w == 0) STAMP(6);
  }
  lds_wait(&L.ctrl[C_OPUB], 4);
  __builtin_amdgcn_s_setprio(2);
  {
    const u32 oo = WS_OP + (u32)(((h_o * 4 + w) * 8 + 4 * (lane >> 5)) * 512 + (lane & 31) * 16);
    u32x4 od[4];
    bool need[4] = {true, true, true, true};
#pragma nounroll
    for (int it = 0; it < GLB_SPIN_CAP; it++) {
      CBAR();
#pragma unroll
      for (int i = 0; i < 4; i++)
        if (need[i]) od[i] = ld_sc1(rws, oo + i * 512, 0);
#pragma unroll
      for (int i = 0; i < 4; i++) need[i] = umax4(od[i]) == SENT;
      if (!__any(need[0] | need[1] | need[2] | need[3])) break;
      __builtin_amdgcn_s_sleep(1);
    }
    CBAR();
    if (w == 0) STAMP(15);
    u32x4 of;
#pragma unroll
    for (int i = 0; i < 4; i++)
      of[i] = pack2((w0 * bflo(od[0][i]) + w1 * bflo(od[1][i])) + (w2 * bflo(od[2][i]) + w3 * bflo(od[3][i])),
                    (w0 * bfhi(od[0][i]) + w1 * bfhi(od[1][i])) + (w2 * bfhi(od[2][i]) + w3 * bfhi(od[3][i])));
    L.ofr[w * 64 + (lane & 3) * 16 + (lane >> 2)] = of;
  }
  lds_signal(&L.ctrl[C_OF], lane);
  lds_wait<0>(&L.ctrl[C_OF], 4);
  {
    f32x4 acc0 = zero4, acc1 = zero4;
#pragma unroll
    for (int kq = 0; kq < 4; kq++) {
      const u32x4 of = L.ofr[kq * 64 + lane];
      acc0 = mfma16(F[kq][0], of, acc0);
      acc1 = mfma16(F[kq][1], of, acc1);
    }
    const bool hi = (lane >> 3) & 1;
    f32x4 r;
#pragma unroll
    for (int i = 0; i < 4; i++) r[i] = (hi ? acc1[i] : acc0[i]) + dpp<XOR8>(hi ? acc0[i] : acc1[i]);
    const int cc = ct_o * 32 + 8 * w + (hi ? 4 : 0) + g;
    st_sc1(rws, WS_FP + (u32)((cc * 64 + h_o) * 128 + (lane & 7) * 16), enc(r, T));
  }
  lds_signal(&L.ctrl[C_O3], lane);
  if (w == 0) STAMP(16);
}

extern "C" void kernel_launch(void* const* d_in, const int* in_sizes, int n_in,
                              void* d_out, int out_size, void* d_ws, size_t ws_size,
                              hipStream_t stream) {
  fused_decode_kernel<<<256, 832, 0, stream>>>((const u16*)d_in[0], (const u16*)d_in[1], (const u16*)d_in[2],
                                               (const u16*)d_in[3], (const u16*)d_in[4], (const u16*)d_in[6],
                                               (const u16*)d_in[7], (const u16*)d_in[8], (u16*)d_out,
                                               (unsigned char*)d_ws);
}
```

```cpp
#include <hip/hip_runtime.h>
#include <stdint.h>

typedef unsigned int u32;
typedef unsigned long long u64;
typedef unsigned short u16;
typedef __attribute__((ext_vector_type(4))) unsigned int u32x4;
typedef __attribute__((ext_vector_type(2))) unsigned int u32x2;
typedef __attribute__((ext_vector_type(16))) unsigned int u32x16;
typedef __attribute__((ext_vector_type(4))) float f32x4;
typedef __attribute__((ext_vector_type(2))) float f32x2;
typedef __attribute__((ext_vector_type(8))) __bf16 bf16x8;
typedef __attribute__((ext_vector_type(2))) __bf16 bf16x2;
typedef __amdgpu_buffer_rsrc_t rsrc_t;

constexpr u32 KV_BYTES = 8u * 4u * 4096u * 128u * 2u;
constexpr u32 WQ_BYTES = 1024u * 4096u * 2u;
constexpr u32 WG_BYTES = 1024u * 3584u * 2u;

constexpr u32 WS_EPOCH = 0;
constexpr u32 WS_TK = 1024;
constexpr u32 WS_QP = 4096;
constexpr u32 WS_GP = WS_QP + 524288u;
constexpr u32 WS_OP = WS_QP;
constexpr u32 WS_ST = WS_GP + 112u * 8192u;
constexpr u32 WS_QD = WS_ST + 32768u;
constexpr u32 WS_QF = WS_QD + 4096u;
constexpr u32 WS_H = WS_QF + 131072u;
constexpr u32 WS_FP = WS_H + 114688u;
constexpr u32 WS_END = WS_FP + 256u * 8192u;

#define LSTR "sc1"
#define NSTR "nt"
constexpr int AUX_NT = 2, AUX_SC1 = 16;
constexpr u32 TMASK = 0x3FFu;
constexpr int LDS_SPIN_CAP = 1 << 21;
constexpr int GLB_SPIN_CAP = 1 << 17;

enum { C_LN = 0, C_CL, C_P1Q, C_P1G, C_P1I, C_X, C_Q, C_KI, C_M, C_P, C_VI, C_G, C_OPUB, C_QD, C_F1, C_ED, C_EI,
       C_U, C_U1, C_LD, C_OF, C_OF1, C_ST, C_ST1, C_N };

#define CBAR() asm volatile("" ::: "memory")
#define WIN 6
#define WINV 8
#define WIN1A 4
#define WIN1 8
#define WINE 8
#define NTHR 960
#define QO2 3
#define QSL 2
#define QDEP 12
#define OSL 1
#define QT1 280
#define QT2 60
#define PVSL 1
#define PVPRIO 2
#define DEFT 1050
#define TK1 9
#define TK2 8
#define NPOOL 4
constexpr u32 ND = 112 / NPOOL, NU = 240 / NPOOL;
#define WSLEEP 2
#define PSLEEP 4
template <int N> __device__ __forceinline__ void wait_vm() { asm volatile("s_waitcnt vmcnt(%0)" ::"n"(N) : "memory"); }

__device__ __forceinline__ rsrc_t mk_rsrc(const void* p, u32 bytes) {
  return __builtin_amdgcn_make_buffer_rsrc((void*)p, (short)0, (int)bytes, 0x00020000);
}
__device__ __forceinline__ u32x4 ld_nt(rsrc_t r, u32 voff, u32 soff) { return __builtin_amdgcn_raw_buffer_load_b128(r, (int)voff, (int)soff, AUX_NT); }
__device__ __forceinline__ u32x4 ld_sc1(rsrc_t r, u32 voff, u32 soff) { return __builtin_amdgcn_raw_buffer_load_b128(r, (int)voff, (int)soff, AUX_SC1); }
__device__ __forceinline__ void st_sc1(rsrc_t r, u32 voff, u32x4 v) { __builtin_amdgcn_raw_buffer_store_b128(v, r, (int)voff, 0, AUX_SC1); }
__device__ __forceinline__ void dma16(u32x4 srd, u32 voff, u32 soff, u32 ldsaddr) {
  u32 keep;
  asm volatile("s_mov_b32 %0, m0\n\ts_mov_b32 m0, %4\n\ts_nop 0\n\tbuffer_load_dwordx4 %1, %2, %3 offen " NSTR " lds\n\ts_mov_b32 m0, %0"
               : "=&s"(keep) : "v"(voff), "s"(srd), "s"(soff), "s"(ldsaddr) : "memory");
}
__device__ __forceinline__ u32x4 mk_srd(const void* p, u32 bytes) {
  const u64 a = (u64)p;
  return u32x4{(u32)a, (u32)(a >> 32) & 0xFFFFu, bytes, 0x00020000u};
}
typedef short v4i16_t __attribute__((ext_vector_type(4)));
typedef __attribute__((address_space(3))) unsigned char* lds_ptr;
__device__ __forceinline__ u32x4 tr_frag(lds_ptr p0, lds_ptr p1) {
  const u32x2 a = __builtin_bit_cast(u32x2, __builtin_amdgcn_ds_read_tr16_b64_v4i16((__attribute__((address_space(3))) v4i16_t*)p0));
  const u32x2 b = __builtin_bit_cast(u32x2, __builtin_amdgcn_ds_read_tr16_b64_v4i16((__attribute__((address_space(3))) v4i16_t*)p1));
  return u32x4{a[0], a[1], b[0], b[1]};
}

__device__ __forceinline__ float u2f(u32 x) { return __uint_as_float(x); }
__device__ __forceinline__ u32 f2u(float x) { return __float_as_uint(x); }
__device__ __forceinline__ float bflo(u32 x) { return u2f(x << 16); }
__device__ __forceinline__ float bfhi(u32 x) { return u2f(x & 0xFFFF0000u); }
__device__ __forceinline__ u32 pack2(float lo, float hi) {
  bf16x2 v = {(__bf16)lo, (__bf16)hi};
  return __builtin_bit_cast(u32, v);
}
__device__ __forceinline__ float rbf(float x) { return (float)(__bf16)x; }
template <int CTRL> __device__ __forceinline__ float dpp(float x) {
  return __builtin_bit_cast(float, __builtin_amdgcn_mov_dpp(__builtin_bit_cast(int, x), CTRL, 0xf, 0xf, true));
}
constexpr int XOR1 = 0xB1, XOR2 = 0x4E, XOR7 = 0x141, XOR8 = 0x128;
__device__ __forceinline__ float x32_max(float x) {
  auto t = __builtin_amdgcn_permlane32_swap(f2u(x), f2u(x), false, false);
  return fmaxf(u2f(t[0]), u2f(t[1]));
}
__device__ __forceinline__ float x32_sum(float x) {
  auto t = __builtin_amdgcn_permlane32_swap(f2u(x), f2u(x), false, false);
  return u2f(t[0]) + u2f(t[1]);
}
__device__ __forceinline__ float xrow_max(float x) {
  auto s = __builtin_amdgcn_permlane16_swap(f2u(x), f2u(x), false, false);
  return x32_max(fmaxf(u2f(s[0]), u2f(s[1])));
}
__device__ __forceinline__ float xrow_sum(float x) {
  auto s = __builtin_amdgcn_permlane16_swap(f2u(x), f2u(x), false, false);
  return x32_sum(u2f(s[0]) + u2f(s[1]));
}
__device__ __forceinline__ float half_sum(float x) {
  x += dpp<XOR1>(x); x += dpp<XOR2>(x); x += dpp<XOR7>(x); x += dpp<XOR8>(x);
  auto s = __builtin_amdgcn_permlane16_swap(f2u(x), f2u(x), false, false);
  return u2f(s[0]) + u2f(s[1]);
}
__device__ __forceinline__ float fdiv(float a, float d, float inv) {
  const float q = a * inv;
  return __builtin_fmaf(__builtin_fmaf(-q, d, a), inv, q);
}
__device__ __forceinline__ f32x4 mfma16(u32x4 a, u32x4 b, f32x4 c) {
  return __builtin_amdgcn_mfma_f32_16x16x32_bf16(__builtin_bit_cast(bf16x8, a), __builtin_bit_cast(bf16x8, b), c, 0, 0, 0);
}
__device__ __forceinline__ u32x4 tcol(const u32x4* R, int t) {
  const u32 sel = (t & 1) ? 0x07060302u : 0x05040100u;
  const int d = t >> 1;
  u32x4 r;
  r[0] = __builtin_amdgcn_perm(R[1][d], R[0][d], sel);
  r[1] = __builtin_amdgcn_perm(R[3][d], R[2][d], sel);
  r[2] = __builtin_amdgcn_perm(R[5][d], R[4][d], sel);
  r[3] = __builtin_amdgcn_perm(R[7][d], R[6][d], sel);
  return r;
}
__device__ __forceinline__ u32x4 enc(f32x4 v, u32x4 T) {
  u32x4 r;
#pragma unroll
  for (int i = 0; i < 4; i++) r[i] = (f2u(v[i]) & ~TMASK) | (T[i] & TMASK);
  return r;
}
__device__ __forceinline__ float dec1(u32 w) { return u2f(__builtin_amdgcn_bitop3_b32(w, TMASK, 0x200u, 0xBA)); }
__device__ __forceinline__ f32x4 dec(u32x4 w) { return f32x4{dec1(w[0]), dec1(w[1]), dec1(w[2]), dec1(w[3])}; }
__device__ __forceinline__ u32 tdiff(u32x4 w, u32x4 T) { return ((w[0] ^ T[0]) | (w[1] ^ T[1])) | ((w[2] ^ T[2]) | (w[3] ^ T[3])); }

#define FSL 2
#define FTHR 64
#define FSLS 4
#define PDEPTH 24
#define P_LOAD(n, r, o) "s_mov_b64 exec, %[" n "]\n\tbuffer_load_dwordx4 " r ", %[off], %[srd], 0 offen offset:%[" o "] " LSTR "\n\t"
#define P_CHK(n, a, b, c, d, cmp)                                                                             \
  "v_xor_b32 %[t0], %[T0], " a "\n\tv_bitop3_b32 %[t0], " b ", %[T1], %[t0] bitop3:0xbe\n\t"                  \
  "v_bitop3_b32 %[t0], " c ", %[T2], %[t0] bitop3:0xbe\n\tv_bitop3_b32 %[t0], " d ", %[T3], %[t0] bitop3:0xbe\n\t" \
  "v_and_b32 %[t0], %[M], %[t0]\n\t" cmp " vcc, 0, %[t0]\n\ts_and_b64 %[" n "], %[" n "], vcc\n\t"
#define P_TAG(n, a, b, c, d) P_CHK(n, a, b, c, d, "v_cmp_ne_u32")
#define P_BODY(CHK)                                                                                           \
  "s_mov_b64 %[sv], exec\n\ts_mov_b32 %[cnt], %[cap]\n"                            \
  "1:\n\t" P_LOAD("n0", "v[112:115]", "o0") P_LOAD("n1", "v[116:119]", "o1") P_LOAD("n2", "v[120:123]", "o2") \
  P_LOAD("n3", "v[124:127]", "o3")                                                                            \
  "s_mov_b64 exec, %[sv]\n\ts_cmp_gt_u32 %[c0], %[thr]\n\ts_cbranch_scc0 2f\n\ts_waitcnt vmcnt(0)\n\ts_branch 3f\n" \
  "2:\n\ts_sleep %[sl]\n\ts_waitcnt vmcnt(%[dep])\n"                                                          \
  "3:\n\t" CHK("n0", "v112", "v113", "v114", "v115") CHK("n1", "v116", "v117", "v118", "v119")                \
  CHK("n2", "v120", "v121", "v122", "v123") CHK("n3", "v124", "v125", "v126", "v127")                         \
  "s_bcnt1_i32_b64 %[c0], %[n0]\n\ts_bcnt1_i32_b64 %[c1], %[n1]\n\ts_add_u32 %[c0], %[c0], %[c1]\n\t"         \
  "s_bcnt1_i32_b64 %[c1], %[n2]\n\ts_add_u32 %[c0], %[c0], %[c1]\n\ts_bcnt1_i32_b64 %[c1], %[n3]\n\t"         \
  "s_add_u32 %[c0], %[c0], %[c1]\n\ts_cmp_eq_u32 %[c0], 0\n\ts_cbranch_scc1 4f\n\t"                           \
  "s_cmp_gt_u32 %[c0], %[thr]\n\ts_cbranch_scc0 5f\n\ts_sleep %[sls]\n"                                     \
  "5:\n\ts_sub_u32 %[cnt], %[cnt], 1\n\ts_cbranch_scc0 1b\n"                                                        \
  "4:"
template <int STR, int SL, int THR, int SLS>
__device__ __forceinline__ void poll4(u32x4 srd, u32 off, u32x4* r, u64 n0, u64 n1, u64 n2, u64 n3, u32x4 T) {
  u64 sv;
  u32 cnt, c1, t0;
  u32 c0 = __builtin_popcountll(n0) + __builtin_popcountll(n1) + __builtin_popcountll(n2) + __builtin_popcountll(n3);
#define P_OPS                                                                                                  \
  : "+{v[112:115]}"(r[0]), "+{v[116:119]}"(r[1]), "+{v[120:123]}"(r[2]), "+{v[124:127]}"(r[3]), [n0] "+s"(n0), \
    [n1] "+s"(n1), [n2] "+s"(n2), [n3] "+s"(n3), [sv] "=&s"(sv), [cnt] "=&s"(cnt), [c0] "+s"(c0), [c1] "=&s"(c1), \
    [t0] "=&v"(t0)                                                                                              \
  : [off] "v"(off), [srd] "s"(srd), [T0] "s"(T[0]), [T1] "s"(T[1]), [T2] "s"(T[2]), [T3] "s"(T[3]), [M] "v"(TMASK), \
    [cap] "n"(GLB_SPIN_CAP), [thr] "n"(THR), [sl] "n"(SL), [sls] "n"(SLS), [dep] "n"(PDEPTH), [o0] "n"(0), [o1] "n"(STR),       \
    [o2] "n"(2 * STR), [o3] "n"(3 * STR)                                                                        \
  : "vcc", "scc", "memory"
  asm volatile(P_BODY(P_TAG) P_OPS);
}
#define Q_LOAD(n, r, o) "s_mov_b64 exec, %[" n "]\n\tbuffer_load_dwordx4 " r ", %[off], %[srd], " o " offen " LSTR "\n\t"
template <int SL, int K, int DEP>
__device__ __forceinline__ void pollq(u32x4 srd, u32 off, u32 str, u32x4* r, u64 n0, u64 n1, u64 n2, u64 n3, u32x4 T) {
  u64 sv;
  u32 cnt, c1, t0;
  asm volatile("s_mov_b64 %[sv], exec\n\ts_mov_b32 %[cnt], %[cap]\n"
               "1:\n\ts_waitcnt vmcnt(%[dep])\n\t" Q_LOAD("n0", "v[112:115]", "0") Q_LOAD("n1", "v[116:119]", "%[s1]")
               Q_LOAD("n2", "v[120:123]", "%[s2]") Q_LOAD("n3", "v[124:127]", "%[s3]")
               "s_mov_b64 exec, %[sv]\n\ts_mov_b32 %[c1], %[k]\n"
               "2:\n\ts_sleep %[sl]\n\t" P_TAG("n0", "v112", "v113", "v114", "v115") P_TAG("n1", "v116", "v117", "v118", "v119")
               P_TAG("n2", "v120", "v121", "v122", "v123") P_TAG("n3", "v124", "v125", "v126", "v127")
               "s_or_b64 vcc, %[n0], %[n1]\n\ts_or_b64 vcc, vcc, %[n2]\n\ts_or_b64 vcc, vcc, %[n3]\n\ts_cbranch_scc0 4f\n\t"
               "s_sub_u32 %[c1], %[c1], 1\n\ts_cbranch_scc0 2b\n\t"
               "s_sub_u32 %[cnt], %[cnt], 1\n\ts_cbranch_scc0 1b\n"
               "4:"
               : "+{v[112:115]}"(r[0]), "+{v[116:119]}"(r[1]), "+{v[120:123]}"(r[2]), "+{v[124:127]}"(r[3]), [n0] "+s"(n0),
                 [n1] "+s"(n1), [n2] "+s"(n2), [n3] "+s"(n3), [sv] "=&s"(sv), [cnt] "=&s"(cnt), [c1] "=&s"(c1), [t0] "=&v"(t0)
               : [off] "v"(off), [srd] "s"(srd), [T0] "s"(T[0]), [T1] "s"(T[1]), [T2] "s"(T[2]), [T3] "s"(T[3]), [M] "v"(TMASK),
                 [cap] "n"(GLB_SPIN_CAP), [sl] "n"(SL), [k] "n"(K), [dep] "n"(DEP), [s1] "s"(str), [s2] "s"(2 * str), [s3] "s"(3 * str)
               : "vcc", "scc", "memory");
}
__device__ __forceinline__ void poll_drain(const u32x4* r) {
  asm volatile("s_waitcnt vmcnt(0)" ::"{v[112:115]}"(r[0]), "{v[116:119]}"(r[1]), "{v[120:123]}"(r[2]), "{v[124:127]}"(r[3]) : "memory");
}

template <int SL = 1> __device__ __forceinline__ void lds_wait(u32* p, u32 target) {
  CBAR();
#pragma nounroll
  for (int i = 0; i < LDS_SPIN_CAP; i++) {
    u32 v = __hip_atomic_load(p, __ATOMIC_RELAXED, __HIP_MEMORY_SCOPE_WORKGROUP);
    if (__builtin_amdgcn_readfirstlane(v) >= target) break;
    __builtin_amdgcn_s_sleep(SL);
  }
  CBAR();
}
__device__ __forceinline__ u32 lds_arrive(u32* p, int lane) {
  asm volatile("s_waitcnt lgkmcnt(0)" ::: "memory");
  u32 old = 0;
  if (lane == 0) old = __hip_atomic_fetch_add(p, 1u, __ATOMIC_RELAXED, __HIP_MEMORY_SCOPE_WORKGROUP);
  old = __builtin_amdgcn_readfirstlane(old);
  CBAR();
  return old;
}
__device__ __forceinline__ void lds_signal(u32* p, int lane) {
  asm volatile("s_waitcnt lgkmcnt(0)" ::: "memory");
  if (lane == 0) __hip_atomic_fetch_add(p, 1u, __ATOMIC_RELAXED, __HIP_MEMORY_SCOPE_WORKGROUP);
  CBAR();
}

__device__ __forceinline__ u32 lds_addr(const void* p) { return (u32)(uintptr_t)(lds_ptr)p; }
__device__ __forceinline__ void lds_post(u32* p) {
  u64 sv;
  asm volatile("s_mov_b64 %0, exec\n\ts_mov_b64 exec, 1\n\tds_add_u32 %1, %2\n\ts_mov_b64 exec, %0"
               : "=&s"(sv) : "v"(lds_addr(p)), "v"(1u) : "memory");
}
template <class X> __device__ __forceinline__ void pin(X& x) { asm volatile("" : "+v"(x)); }

constexpr int XN_STRIDE = 1024 + 8;
constexpr int SL_STRIDE = 68;
constexpr int Q_STRIDE = 136;
constexpr int OV_XN = 65536, OV_SQ = OV_XN + 8 * XN_STRIDE * 2, OV_SG = OV_SQ + 4 * 8 * SL_STRIDE * 4;
static_assert(OV_SG + 4 * 8 * SL_STRIDE * 4 <= 131072, "overlay");

struct __align__(16) Lds {
  unsigned char img[131072];
  u32x4 P[16 * 4 * 8];
  u32x4 bfr[2][32 * 8];
  u16 qs[8 * Q_STRIDE];
  float wm[64];
  float wl[64];
  float wgt[2][64];
  float gate[64];
  u32 ctrl[32];
  u32 vprog[4];
  u32 pvprog[4];
  u32 eprog[2][4];
  u32 tag[4];
};

__device__ __forceinline__ void w_epilogue(const f32x4* acc, float* slab, int lane) {
  float val[2][8];
#pragma unroll
  for (int t = 0; t < 8; t++) {
#pragma unroll
    for (int r = 0; r < 2; r++) {
      auto s = __builtin_amdgcn_permlane32_swap(f2u(acc[t][r]), f2u(acc[t][r + 2]), false, false);
      val[r][t] = u2f(s[0]) + dpp<XOR8>(u2f(s[1]));
    }
  }
  if (!(lane & 8)) {
    const int b0 = 4 * ((lane >> 4) & 1) + 2 * (lane >> 5);
    const int cp = lane & 7;
#pragma unroll
    for (int r = 0; r < 2; r++) {
      float* p = slab + (b0 + r) * SL_STRIDE + 8 * cp;
      *(f32x4*)p = f32x4{val[r][0], val[r][1], val[r][2], val[r][3]};
      *(f32x4*)(p + 4) = f32x4{val[r][4], val[r][5], val[r][6], val[r][7]};
    }
  }
}
__device__ __forceinline__ f32x4 slab_sum(const float* slab, int b, int col) {
  const float* p = slab + b * SL_STRIDE + col;
  f32x4 a = *(const f32x4*)p;
  f32x4 bb = *(const f32x4*)(p + 8 * SL_STRIDE);
  f32x4 c = *(const f32x4*)(p + 16 * SL_STRIDE);
  f32x4 d = *(const f32x4*)(p + 24 * SL_STRIDE);
  return (a + bb) + (c + d);
}
__device__ __forceinline__ u32x4 read_tag(const u32* p) {
  u32x4 t = *(const u32x4*)p;
  t[0] = __builtin_amdgcn_readfirstlane(t[0]); t[1] = __builtin_amdgcn_readfirstlane(t[1]);
  t[2] = __builtin_amdgcn_readfirstlane(t[2]); t[3] = __builtin_amdgcn_readfirstlane(t[3]);
  return t;
}
__device__ __forceinline__ u32 prog_min(const u32* p) {
  u32x4 t;
  asm volatile("ds_read_b128 %0, %1\n\ts_waitcnt lgkmcnt(0)" : "=v"(t) : "v"(lds_addr(p)) : "memory");
  return __builtin_amdgcn_readfirstlane(min(min(t[0], t[1]), min(t[2], t[3])));
}
template <int SL = 1> __device__ __forceinline__ void prog_wait(const u32* p, u32 target) {
#pragma nounroll
  for (int i = 0; i < LDS_SPIN_CAP; i++) {
    if (prog_min(p) >= target) break;
    __builtin_amdgcn_s_sleep(SL);
  }
}
__device__ __forceinline__ void prog_set(u32* p, u32 val, int lane) {
  CBAR();
  if (lane == 0) __hip_atomic_store(p, val, __ATOMIC_RELAXED, __HIP_MEMORY_SCOPE_WORKGROUP);
  CBAR();
}
__device__ __forceinline__ void defer_until(u64 t0, u32 ticks) {
#pragma nounroll
  for (int i = 0; i < 4096; i++) {
    if ((u32)(__builtin_amdgcn_s_memrealtime() - t0) >= ticks) break;
    __builtin_amdgcn_s_sleep(2);
  }
}
__device__ __forceinline__ void tile_frags(lds_ptr b0, lds_ptr b1, int j, u32x4* F) {
  F[0] = tr_frag(b0 + j * 1024, b0 + j * 1024 + 4096);
  F[1] = tr_frag(b1 + j * 1024, b1 + j * 1024 + 4096);
  F[2] = tr_frag(b0 + j * 1024 + 8192, b0 + j * 1024 + 12288);
  F[3] = tr_frag(b1 + j * 1024 + 8192, b1 + j * 1024 + 12288);
}
typedef __attribute__((address_space(1))) u64 gu64;
__device__ __forceinline__ u32 unit_of(u32 t) {
  return t >= NU ? NU : (t < NU - ND ? t + ND : t - (NU - ND));
}
__device__ __forceinline__ u32 unit_d(u32 pool, u32 tk) {
  static_assert(NPOOL == 4, "pools");
  const u32 n = tk < 7 ? pool * 7 + tk : 28 + pool * 21 + (tk - 7);
  const u32 ci = (n * 147) >> 11;
  return (n - 14 * ci) + 16 * ((0x76432051u >> (4 * ci)) & 7);
}
__device__ __forceinline__ u32 unit_row(u32 pool, u32 tk) {
  if (tk < ND) return unit_d(pool, tk) & 15;
  return pool * (32 / NPOOL) + (((tk - ND) >> 1) & (32 / NPOOL - 1));
}
__device__ __forceinline__ u32 unit_col(u32 pool, u32 tk) {
  if (tk < ND) return unit_d(pool, tk) >> 4;
  return (((tk - ND) & 1) << 1) | ((tk - ND) >= 64 / NPOOL ? 1 : 0);
}

__device__ __forceinline__ const void* ptr2(u32 lo, u32 hi) { return (const void*)(((u64)hi << 32) | lo); }
__device__ __forceinline__ u32 opaque(u32 v) { asm volatile("" : "+s"(v)); return v; }

__global__ void __launch_bounds__(NTHR, 4)
fused_decode_kernel(const u16* e_x, const u16* e_wq, const u16* e_wg, const u16* e_wu, unsigned char* e_ws, const u16*, const u16*,
                    const u16*, const u16*, u16*) {
  __shared__ Lds L;
  u32x16 ka;
  u32x4 kb;
#define ARGS_RELOAD()                                                                                          \
  asm volatile("s_load_dwordx16 %0, %2, 0x0\n\ts_load_dwordx4 %1, %2, 0x40\n\ts_waitcnt lgkmcnt(0)"         \
               : "=&s"(ka), "=&s"(kb) : "s"(__builtin_amdgcn_kernarg_segment_ptr()) : "memory")
#define P_X ptr2(ka[0], ka[1])
#define P_WS ptr2(ka[8], ka[9])
#define P_K ptr2(ka[10], ka[11])
#define P_V ptr2(ka[12], ka[13])
#define P_OUT ptr2(kb[2], kb[3])
  const int tid = threadIdx.x;
  const int lane = tid & 63;
  const int wave = __builtin_amdgcn_readfirstlane(tid >> 6);
  u16* xn = (u16*)(L.img + OV_XN);
  const int g = lane >> 4;
  const lds_ptr img = (lds_ptr)L.img;
  const u32 img_addr = (u32)(uintptr_t)img;
  const f32x4 zero4 = {0.f, 0.f, 0.f, 0.f};
#define UNIT_MAP(c)                                                                       \
  const int xi = (c) & 7, ji = (c) >> 3;                                                  \
  const bool special = (xi & 1) && ji >= 17;                                              \
  const int sidx = (ji - 17) * 4 + (xi >> 1);                                   \
  const int nidx = (xi >> 1) * ((xi & 1) ? 17 : 32) + (xi & 1) * 128 + ji;
#define ATT_MAP(c)                                                                        \
  const int hkv = (c) >> 6, ab = ((c) >> 3) & 7, as = (c) & 7;       \
  const u32 kvbase = (u32)((ab * 4 + hkv) * 4096 + as * 512) * 256u;

  if (wave >= 12) {
    if (wave == 12) {
      if (lane < 48) L.ctrl[lane] = 0;
      ka[8] = (u32)(u64)e_ws; ka[9] = (u32)((u64)e_ws >> 32);
      const rsrc_t rws = mk_rsrc(P_WS, WS_END);
      u32x4 ep;
      asm volatile("s_waitcnt lgkmcnt(0)\n\ts_load_dwordx4 %0, %1, 0x0\n\ts_barrier\n\ts_waitcnt lgkmcnt(0)" : "=&s"(ep) : "s"(P_WS) : "memory");
      const int c = opaque(blockIdx.x);
      u32x4 T;
      {
        u64 ea = ((u64)ep[1] << 32) | ep[0], eb = ((u64)ep[3] << 32) | ep[2];
        u64 h1 = (ea ^ (eb * 0x9E3779B97F4A7C15ull)) * 6364136223846793005ull + 1442695040888963407ull;
        h1 ^= h1 >> 29; h1 *= 0xBF58476D1CE4E5B9ull; h1 ^= h1 >> 32;
        u64 h2 = (h1 + eb) * 0x94D049BB133111EBull + 0x2545F4914F6CDD1Dull;
        h2 ^= h2 >> 31;
        u32x4 H = {(u32)h1, (u32)(h1 >> 32), (u32)h2, (u32)(h2 >> 32)};
#pragma unroll
        for (int i = 0; i < 4; i++) T[i] = (H[i] & ~TMASK) | ((ep[i] + 1u + (H[i] & 0x1FFu)) & TMASK);
        if (lane == 0) *(u32x4*)L.tag = T;
      }
      lds_signal(&L.ctrl[C_CL], lane);
      if (c < NPOOL && lane == 0) {
        gu64* tw = (gu64*)((u64)P_WS + WS_TK + c * 256);
        __hip_atomic_exchange(tw, ((u64)T[0] << 32) | (T[1] & ~TMASK), __ATOMIC_RELAXED, __HIP_MEMORY_SCOPE_AGENT);
      }
      {
        const u64 t1 = __builtin_amdgcn_s_memrealtime();
        const u32 o1 = WS_QP + (u32)((c >> 3) * 16384 + (lane >> 4) * 2048 + (c & 7) * 256 + (lane & 15) * 16);
        const u32 o2 = WS_QF + (u32)((c >> 3) * 4096 + lane * 16);
        defer_until(t1, QT1);
        u32x4 q[4];
        bool need[2] = {true, true};
        const u32x4 sw = mk_srd(P_WS, WS_END);
#pragma nounroll
        for (int it = 0; it < GLB_SPIN_CAP; it++) {
          CBAR();
#pragma unroll
          for (int i = 0; i < 2; i++)
            if (need[i]) q[i] = ld_sc1(rws, o1, i * 8192);
          __builtin_amdgcn_sched_barrier(0);
#pragma unroll
          for (int i = 0; i < 2; i++) need[i] = (tdiff(q[i], T) & TMASK) != 0;
          if (!__any(need[0] | need[1])) break;
          __builtin_amdgcn_s_sleep(PSLEEP);
        }
        CBAR();
        f32x4 qs = dec(q[0]) + dec(q[1]);
#pragma unroll
        for (int i = 0; i < 4; i++) qs[i] = rbf(x32_sum(qs[i]));
        if (lane < 33) st_sc1(rws, lane < 32 ? WS_QF + (u32)(c * 512 + lane * 16) : WS_QD + (u32)c * 16u, lane < 32 ? enc(qs, T) : T);
        defer_until(__builtin_amdgcn_s_memrealtime(), QT2);
        q[0] = q[1] = q[2] = q[3] = ~T;
        pollq<QSL, QO2, QDEP>(sw, o2, 1024u, q, ~0ull, ~0ull, ~0ull, ~0ull, T);
#pragma unroll
        for (int i = 0; i < 4; i++)
          *(u32x2*)&L.qs[(2 * i + (lane >> 5)) * Q_STRIDE + 4 * (lane & 31)] =
              u32x2{__builtin_amdgcn_perm(q[i][1], q[i][0], 0x07060302u), __builtin_amdgcn_perm(q[i][3], q[i][2], 0x07060302u)};
        lds_signal(&L.ctrl[C_Q], lane);
        poll_drain(q);
        const int kq = c;
        if (kq < 112) {
          const u32 ho = WS_GP + (u32)(kq * 8192 + lane * 16);
          const bool fine = (kq & 14) == 6;
          const bool hi = (lane >> 3) & 1;
          u32x4 p[8];
          bool nh[8];
#pragma unroll
          for (int i = 0; i < 8; i++) { p[i] = u32x4{0u, 0u, 0u, 0u}; nh[i] = i < 4 || fine; }
#pragma nounroll
          for (int it = 0; it < GLB_SPIN_CAP; it++) {
            CBAR();
#pragma unroll
            for (int i = 0; i < 8; i++)
              if (nh[i]) p[i] = ld_sc1(rws, ho, i * 1024);
            __builtin_amdgcn_sched_barrier(0);
            bool any = false;
#pragma unroll
            for (int i = 0; i < 8; i++) { nh[i] = (i < 4 || fine) && (tdiff(p[i], T) & TMASK) != 0; any |= nh[i]; }
            if (!__any(any)) break;
            __builtin_amdgcn_s_sleep(PSLEEP);
          }
          CBAR();
          const f32x4 S0 = (dec(p[0]) + dec(p[2])) + (dec(p[4]) + dec(p[6]));
          const f32x4 S1 = (dec(p[1]) + dec(p[3])) + (dec(p[5]) + dec(p[7]));
          f32x4 hv;
#pragma unroll
          for (int i = 0; i < 4; i++) {
            const float own = rbf(hi ? S1[i] : S0[i]);
            const float oth = dpp<XOR8>(rbf(hi ? S0[i] : S1[i]));
            const float gg = hi ? oth : own, uu = hi ? own : oth;
            hv[i] = rbf(rbf(1.f / (1.f + __expf(-gg))) * uu);
          }
          st_sc1(rws, WS_H + (u32)(kq * 1024 + lane * 16), enc(hv, T));
        }
      }
      {
        const u32 qdo = WS_QD + (u32)(((c >> 3) & (NPOOL - 1)) * 1024 + lane * 16);
        u32x4 fl;
        bool need = true;
#pragma nounroll
        for (int it = 0; it < GLB_SPIN_CAP; it++) {
          CBAR();
          if (need) fl = ld_sc1(rws, qdo, 0);
          need = tdiff(fl, T) != 0;
          if (!__any(need)) break;
          __builtin_amdgcn_s_sleep(16);
        }
        CBAR();
        lds_signal(&L.ctrl[C_QD], lane);
      }
      return;
    }
    const u64 t0 = __builtin_amdgcn_s_memrealtime();
    CBAR();
    __builtin_amdgcn_s_barrier();
    CBAR();
    ARGS_RELOAD();
    const int c = opaque(blockIdx.x);
    const int e = wave - 13;
    const int pool = (c >> 3) & (NPOOL - 1);
    lds_wait<8>(&L.ctrl[C_P], 8);
    const u32x4 T = read_tag(L.tag);
    u32 tk = NU;
    bool take = true;
    if (e == 0) {
      prog_wait(L.vprog, TK1);
      defer_until(t0, DEFT);
    } else {
      lds_wait<2>(&L.ctrl[C_U], 1);
      take = __builtin_amdgcn_readfirstlane(L.ctrl[C_U]) <= NU;
      if (take) lds_wait<2>(&L.ctrl[C_EI], 1);
    }
    if (take) {
      gu64* tw = (gu64*)((u64)P_WS + WS_TK + pool * 256);
#pragma nounroll
      for (int it = 0; it < GLB_SPIN_CAP; it++) {
        u64 old = 0;
        if (lane == 0) old = __hip_atomic_fetch_add(tw, 1ull, __ATOMIC_RELAXED, __HIP_MEMORY_SCOPE_AGENT);
        const u32 lo = __builtin_amdgcn_readfirstlane((u32)old), hi = __builtin_amdgcn_readfirstlane((u32)(old >> 32));
        if (hi == T[0] && ((lo ^ T[1]) & ~TMASK) == 0) { tk = unit_of(lo & TMASK); break; }
        __builtin_amdgcn_s_sleep(8);
      }
    }
    prog_set(&L.ctrl[C_U + e], tk + 1, lane);
    return;
  }
  const int a = wave & 3;
  const bool hi = (lane >> 3) & 1;
  int e;
  if (wave >= 4) {
    const int v = wave - 4;
    const bool grpB = v >= 4;
    u32 xfin = 0u;
    if (grpB) {
      const int cb = blockIdx.x;
      UNIT_MAP(cb)
      const bool r3 = !(xi & 1) && (nidx & 3) == 0;
      const int m = (xi & 1) ? (special ? 164 + sidx : nidx - 32) : nidx - 1 - (nidx >> 2);
      const int t = m >> 2, t7 = (t * 37) >> 8, tr = t - 7 * t7;
      const int ks_q = (r3 ? nidx >> 2 : m) & 3;
      const int ct_q = r3 ? (nidx >> 4) * 8 + 3 : t7 * 8 + tr + (tr >= 3 ? 1 : 0);
      const int row = a + 4 * (lane >> 5);
      const u32 xoff = (u32)(row * 2048 + (lane & 31) * 16);
      const u32 qoff = (u32)(16 * g + 8 * ((lane >> 3) & 1)) * 8192u + (lane & 7) * 16;
      const u32 qbase = __builtin_amdgcn_readfirstlane((u32)(ks_q * 256 + a * 64) * 8192u + ct_q * 128);
      const rsrc_t rx = mk_rsrc(e_x, 8u * 1024u * 2u);
      u32x4 xr[4];
#pragma unroll
      for (int i = 0; i < 4; i++) xr[i] = __builtin_amdgcn_raw_buffer_load_b128(rx, xoff + i * 512, 0, 0);
      u32x4 Rq[8];
      const rsrc_t rq = mk_rsrc(e_wq, WQ_BYTES);
#pragma unroll
      for (int i = 0; i < 8; i++) Rq[i] = ld_nt(rq, qoff, qbase + i * 8192);
      __builtin_amdgcn_sched_barrier(0);
      CBAR();
      __builtin_amdgcn_s_barrier();
      CBAR();
      __builtin_amdgcn_s_setprio(2);
      {
        float el[32];
#pragma unroll
        for (int i = 0; i < 4; i++)
#pragma unroll
          for (int j = 0; j < 4; j++) { el[8 * i + 2 * j] = bflo(xr[i][j]); el[8 * i + 2 * j + 1] = bfhi(xr[i][j]); }
        float s = 0.f;
#pragma unroll
        for (int i = 0; i < 32; i++) s += el[i];
        const float mean = half_sum(s) * (1.f / 1024.f);
        float s2 = 0.f;
#pragma unroll
        for (int i = 0; i < 32; i++) { float d = el[i] - mean; s2 += d * d; }
        const float var = half_sum(s2) * (1.f / 1024.f);
        const float mb = rbf(mean), vb = rbf(var);
        const float den = rbf(sqrtf(rbf(vb + 1e-5f)));
        const float inv = 1.f / den;
#pragma unroll
        for (int i = 0; i < 4; i++) {
          u32x4 y;
#pragma unroll
          for (int j = 0; j < 4; j++) {
            const u32 d2 = pack2(el[8 * i + 2 * j] - mb, el[8 * i + 2 * j + 1] - mb);
            y[j] = pack2(fdiv(bflo(d2), den, inv), fdiv(bfhi(d2), den, inv));
          }
          *(u32x4*)&xn[row * XN_STRIDE + (lane & 31) * 8 + i * 256] = y;
        }
      }
      lds_signal(&L.ctrl[C_LN], lane);
      lds_wait(&L.ctrl[C_LN], 4);
      const int r = lane & 15;
      const u32x4 af = *(const u32x4*)&xn[(r & 7) * XN_STRIDE + 16 * g + 8 * (r >> 3) + ks_q * 256 + a * 64];
      f32x4 acc[8];
#pragma unroll
      for (int t = 0; t < 8; t++) acc[t] = mfma16(af, tcol(Rq, t), zero4);
      float* sq = (float*)(L.img + OV_SQ);
      w_epilogue(acc, sq + a * 8 * SL_STRIDE, lane);
      if (lds_arrive(&L.ctrl[C_P1Q], lane) == 3) {
        f32x4 s0 = slab_sum(sq, g, 4 * (lane & 15)), s1 = slab_sum(sq, g + 4, 4 * (lane & 15));
        lds_wait(&L.ctrl[C_CL], 1);
        const u32x4 T = read_tag(L.tag);
        const u32 qo = WS_QP + (u32)(((ct_q >> 4) * 8 + g) * 16384 + (ks_q * 2 + (ct_q & 1)) * 2048 + ((ct_q >> 1) & 7) * 256 + (lane & 15) * 16);
        ARGS_RELOAD();
        const rsrc_t rws = mk_rsrc(P_WS, WS_END);
        st_sc1(rws, qo, enc(s0, T));
        st_sc1(rws, qo + 65536u, enc(s1, T));
        lds_signal(&L.ctrl[C_X], lane);
      }
      __builtin_amdgcn_s_setprio(0);
    } else {
      CBAR();
      __builtin_amdgcn_s_barrier();
      CBAR();
    }
    ARGS_RELOAD();
    const int c = opaque(blockIdx.x);
    const int cs = c;
    if (v <= 1) xfin = __builtin_amdgcn_raw_buffer_load_b16(mk_rsrc(P_X, 8u * 1024u * 2u), (lane & 7) * 2048 + cs * 8 + g * 2, 0, 0);
    lds_wait(&L.ctrl[grpB ? C_KI : C_P1I], 4);
    ATT_MAP(c)
    const rsrc_t rws = mk_rsrc(P_WS, WS_END);
    u32x4 K[16];
    {
      const rsrc_t rk = mk_rsrc(P_K, KV_BYTES);
      const u32 koff = (u32)(((lane >> 2) & 3) * 2048 + g * 64 + (lane & 3) * 16);
#pragma unroll
      for (int n = 0; n < 16; n++) {
        if (n >= WIN) wait_vm<WIN - 1>();
        K[n] = ld_nt(rk, koff, kvbase + v * 16384 + ((n & 1) * 32 + (n >> 1)) * 256);
      }
    }
    __builtin_amdgcn_sched_barrier(0);
    lds_signal(&L.ctrl[C_KI], lane);
    lds_wait(&L.ctrl[C_CL], 1);
    const u32x4 T = read_tag(L.tag);
    lds_wait(&L.ctrl[C_Q], 1);
    __builtin_amdgcn_s_setprio(1);
    float sc[8];
    float Mh;
    {
      const u16* qp = &L.qs[(lane & 7) * Q_STRIDE + 32 * g + 8 * ((lane >> 3) & 1)];
      const u32x4 q0 = *(const u32x4*)qp, q1 = *(const u32x4*)(qp + 16);
      float m = -INFINITY;
#pragma unroll
      for (int mm = 0; mm < 8; mm++) {
        const f32x4 d0 = mfma16(K[2 * mm], q0, zero4), d1 = mfma16(K[2 * mm], q1, zero4);
        const f32x4 e0 = mfma16(K[2 * mm + 1], q0, zero4), e1 = mfma16(K[2 * mm + 1], q1, zero4);
        const float r0 = (d0[0] + d1[2]) + dpp<XOR8>(d0[1] + d1[3]);
        const float r1 = (e0[1] + e1[3]) + dpp<XOR8>(e0[0] + e1[2]);
        const float z = rbf(rbf(hi ? r1 : r0) * 0.08838834764831845f);
        sc[mm] = z;
        m = fmaxf(m, z);
      }
      m = fmaxf(m, dpp<XOR8>(m));
      m = xrow_max(m);
      if (lane < 8) L.wm[lane * 8 + v] = m;
      lds_signal(&L.ctrl[C_M], lane);
      lds_wait(&L.ctrl[C_M], 8);
      const f32x4 m0 = *(const f32x4*)&L.wm[(lane & 7) * 8], m1 = *(const f32x4*)&L.wm[(lane & 7) * 8 + 4];
      Mh = fmaxf(fmaxf(fmaxf(m0[0], m0[1]), fmaxf(m0[2], m0[3])), fmaxf(fmaxf(m1[0], m1[1]), fmaxf(m1[2], m1[3])));
      float ls = 0.f;
#pragma unroll
      for (int i = 0; i < 8; i++) { sc[i] = __expf(sc[i] - Mh); ls += sc[i]; }
      L.P[((2 * v + (hi ? 1 : 0)) * 4 + g) * 8 + (lane & 7)] = u32x4{pack2(sc[0], sc[1]), pack2(sc[2], sc[3]), pack2(sc[4], sc[5]), pack2(sc[6], sc[7])};
      ls += dpp<XOR8>(ls);
      ls = xrow_sum(ls);
      if (lane < 8) L.wl[lane * 8 + v] = ls;
      if (lds_arrive(&L.ctrl[C_P], lane) == 7) {
        const f32x4 l0 = *(const f32x4*)&L.wl[(lane & 7) * 8], l1 = *(const f32x4*)&L.wl[(lane & 7) * 8 + 4];
        const float Ls = ((l0[0] + l0[1]) + (l0[2] + l0[3])) + ((l1[0] + l1[1]) + (l1[2] + l1[3]));
        if (lane < 8) st_sc1(rws, WS_ST + (u32)(c * 128 + lane * 16), u32x4{f2u(Mh), f2u(Ls), T[2], T[3]});
      }
    }
    if (!grpB) {
      const u32 obox = WS_OP + (u32)((c >> 3) * 16384 + (lane & 7) * 2048 + as * 256 + a * 64 + g * 16);
      lds_wait(&L.ctrl[C_P], 8);
      __builtin_amdgcn_s_setprio(PVPRIO);
      f32x4 acc0 = zero4, acc1 = zero4;
      {
        const int q = (lane >> 2) & 3, p = lane & 3;
        const lds_ptr b0 = img + g * 32768 + q * 256 + (((2 * a) ^ (2 * q + (g & 1))) * 32) + p * 8;
        const lds_ptr b1 = img + g * 32768 + q * 256 + (((2 * a + 1) ^ (2 * q + (g & 1))) * 32) + p * 8;
        const u32x4* pp = &L.P[g * 8 + (lane & 7)];
        u32 j = 0;
#pragma nounroll
        for (int it = 0; it < LDS_SPIN_CAP; it++) {
          const u32 avail = prog_min(L.vprog);
          if (avail <= j) { __builtin_amdgcn_s_sleep(PVSL); continue; }
#pragma nounroll
          for (; j < avail; j++) {
            const u32x4 pf = pp[j * 32];
            const u32x4 f0 = tr_frag(b0 + j * 2048, b0 + j * 2048 + 1024);
            const u32x4 f1 = tr_frag(b1 + j * 2048, b1 + j * 2048 + 1024);
            acc0 = mfma16(f0, pf, acc0);
            acc1 = mfma16(f1, pf, acc1);
          }
          if (j >= 16) break;
          asm volatile("s_waitcnt lgkmcnt(0)" ::: "memory");
          prog_set(&L.pvprog[a], j, lane);
        }
      }
      {
        u32x4 ov = {pack2(acc0[0], acc0[1]), pack2(acc0[2], acc0[3]), pack2(acc1[0], acc1[1]), pack2(acc1[2], acc1[3])};
        if ((tdiff(ov, T) & TMASK) == 0) ov[0] ^= 1u;
        if (!(lane & 8)) st_sc1(rws, obox, ov);
      }
      prog_set(&L.pvprog[a], 16, lane);
      if (v >= 2) return;
      const u32 fo = WS_FP + (u32)cs * 8192u + v * 4096 + lane * 16;
      u32x4 f[4];
      f[3] = u32x4{~T[0] & TMASK, 0u, 0u, 0u};
      if (v == 1) {
        lds_wait<2>(&L.ctrl[C_F1], 1);
      } else {
        lds_wait(&L.ctrl[C_ED], 8);
        if (__builtin_amdgcn_readfirstlane(L.ctrl[C_F1]) == 0) {
          const u32 fw = WS_FP + (u32)cs * 8192u + (lane >> 3) * 1024 + (lane & 7) * 16;
#pragma nounroll
          for (int it = 0; it < GLB_SPIN_CAP; it++) {
            CBAR();
            u32x4 t4 = {~T[0], 0u, 0u, 0u};
            if (lane < 32) t4 = ld_sc1(rws, fw, 0);
            if (__any((tdiff(t4, T) & TMASK) == 0)) break;
            __builtin_amdgcn_s_sleep(WSLEEP);
          }
          lds_signal(&L.ctrl[C_F1], lane);
        }
      }
      f[0] = f[1] = f[2] = f[3];
      const u32 mw = opaque((u32)(v - 1));
      const u64 mz = ((u64)mw << 32) | mw;
      poll4<1024, FSL, FTHR, FSLS>(mk_srd(P_WS, WS_END), fo, f, ~0ull, mz | 0xFFFFFFFFFFFFull, mz, mz, T);
      u32 gf;
      float G;
      asm volatile("ds_read_b32 %0, %2\n\tds_read_b32 %1, %3" : "=&v"(gf), "=&v"(G) : "v"(lds_addr(&L.ctrl[C_G])), "v"(lds_addr(&L.wgt[v ^ 1][lane])) : "memory");
      f32x4 S = (dec(f[0]) + dec(f[1])) + (dec(f[2]) + dec(f[3]));
#pragma unroll
      for (int i = 0; i < 4; i++) S[i] += dpp<XOR8>(S[i]);
      const auto s01 = __builtin_amdgcn_permlane16_swap(f2u(S[0]), f2u(S[1]), false, false);
      const auto s23 = __builtin_amdgcn_permlane16_swap(f2u(S[2]), f2u(S[3]), false, false);
      const auto sz = __builtin_amdgcn_permlane32_swap(f2u(u2f(s01[0]) + u2f(s01[1])), f2u(u2f(s23[0]) + u2f(s23[1])), false, false);
      float Z = rbf(u2f(sz[0]) + u2f(sz[1]));
      if (v == 1) {
        Z = rbf(Z / (1.f + __expf(-Z)));
      }
      asm volatile("s_waitcnt lgkmcnt(0)" : "+v"(gf), "+v"(G) :: "memory");
      if (__builtin_amdgcn_readfirstlane(gf) == 0) {
        u64 sv;
        asm volatile("ds_write_b32 %3, %4\n\ts_mov_b64 %2, exec\n\ts_mov_b64 exec, 1\n\tds_add_rtn_u32 %0, %5, %6\n\ts_mov_b64 exec, %2\n\t"
                     "ds_read_b32 %1, %7\n\ts_waitcnt lgkmcnt(0)"
                     : "=&v"(gf), "=&v"(G), "=&s"(sv)
                     : "v"(lds_addr(&L.wgt[v][lane])), "v"(Z), "v"(lds_addr(&L.ctrl[C_G])), "v"(1u), "v"(lds_addr(&L.wgt[v ^ 1][lane])) : "memory");
        if (__builtin_amdgcn_readfirstlane(gf) == 0) {
          poll_drain(f);
          return;
        }
      }
      {
        const u32 y = pack2(bflo(xfin) + rbf(Z * G), 0.f);
        const auto y2 = __builtin_amdgcn_permlane16_swap(y, y, false, false);
        const u32 yp = y2[0] | (y2[1] << 16);
        const auto y4 = __builtin_amdgcn_permlane32_swap(yp, yp, false, false);
        if (lane < 8) __builtin_amdgcn_raw_buffer_store_b64(u32x2{y4[0], y4[1]}, mk_rsrc(P_OUT, 16384u), lane * 2048 + cs * 8, 0, AUX_SC1);
      }
      if (c == 0 && lane == 0) st_sc1(rws, WS_EPOCH, T);
      poll_drain(f);
      return;
    }
    __builtin_amdgcn_s_setprio(0);
    e = 0;
  } else {
    const int w = wave;
    {
      const int cb = blockIdx.x;
      UNIT_MAP(cb)
      const int mat = special ? (sidx + 4) >> 5 : (nidx + 30) >> 7;
      const int rr = nidx - 98 * mat, t = rr >> 1, t7 = (t * 37) >> 8, tr = t - 7 * t7;
      const int ct_g = special ? ((sidx >> 2) - 7 * mat) * 8 + 3 : t7 * 8 + tr + (tr >= 3 ? 1 : 0);
      const int ks_g = special ? sidx & 3 : rr & 1;
      const int krow = (ks_g * 256 + w * 64) * (special ? 1 : 2);
      const bool hasg = !(special && sidx >= 56);
      u32x4 Rg[2][8];
      const u32 goff = (u32)(16 * g + 8 * ((lane >> 3) & 1)) * 7168u + (lane & 7) * 16;
      const u32 gbase = __builtin_amdgcn_readfirstlane((u32)krow * 7168u + ct_g * 128);
      const u32 gb1 = __builtin_amdgcn_readfirstlane(hasg ? WG_BYTES : 0u);
      const void* pg = mat ? e_wu : e_wg;
      const rsrc_t rg = mk_rsrc(pg, gb1);
      const rsrc_t rg2 = mk_rsrc(pg, special ? 0u : WG_BYTES);
#pragma unroll
      for (int n = 0; n < 16; n++) {
        if (n == WIN1A) {
          __builtin_amdgcn_sched_barrier(0);
          CBAR(); __builtin_amdgcn_s_barrier(); CBAR();
        }
        if (n >= WIN1A && n < 8) wait_vm<WIN1A - 1>();
        if (n >= 8 && n >= WIN1) wait_vm<WIN1 - 1>();
        Rg[n >> 3][n & 7] = ld_nt(n < 8 ? rg : rg2, goff, gbase + ((n >> 3) * 64 + (n & 7)) * 7168);
      }
      __builtin_amdgcn_sched_barrier(0);
      lds_signal(&L.ctrl[C_P1I], lane);
      ARGS_RELOAD();
      lds_wait(&L.ctrl[C_LN], 4);
      const int r = lane & 15;
      const u16* xrow = &xn[(r & 7) * XN_STRIDE + 16 * g + 8 * (r >> 3) + krow];
      const u32x4 a0 = *(const u32x4*)xrow;
      const u32x4 a1 = *(const u32x4*)(xrow + (special ? 0 : 64));
      f32x4 acc[8];
#pragma unroll
      for (int t = 0; t < 8; t++) acc[t] = mfma16(a0, tcol(Rg[0], t), zero4);
#pragma unroll
      for (int t = 0; t < 8; t++) acc[t] = mfma16(a1, tcol(Rg[1], t), acc[t]);
      float* sg = (float*)(L.img + OV_SG);
      w_epilogue(acc, sg + w * 8 * SL_STRIDE, lane);
      const u32 old = lds_arrive(&L.ctrl[C_P1G], lane);
      lds_wait(&L.ctrl[C_CL], 1);
      if (old == 3) {
        const u32x4 T = read_tag(L.tag);
        const int b = lane & 7, hg = lane >> 3;
        const f32x4 s0 = slab_sum(sg, b, 4 * hg), s1 = slab_sum(sg, b, 32 + 4 * hg);
        const u32 go = WS_GP + (u32)(((((ct_g * 2) * 4 + ks_g) * 8 + hg) * 2 + mat) * 128 + b * 16);
        if (hasg) {
          const rsrc_t rws = mk_rsrc(P_WS, WS_END);
          st_sc1(rws, go, enc(s0, T));
          st_sc1(rws, go + 8192u, enc(s1, T));
        }
        lds_signal(&L.ctrl[C_X], lane);
      }
    }
    const int c = opaque(blockIdx.x);
    ATT_MAP(c)
    lds_wait<2>(&L.ctrl[C_X], 2);
    lds_wait(&L.ctrl[C_KI], 8);
    {
      const u32x4 sv = mk_srd(P_V, KV_BYTES);
      const int cp = lane & 15;
      const u32 voff = (u32)(g * 256 + (((cp >> 1) ^ (2 * g + (w & 1))) * 32) + (cp & 1) * 16);
      const u32 lb = img_addr + w * 32768;
      wait_vm<0>();
#pragma unroll
      for (int n = 0; n < 32; n++) {
        if (n >= WINV) {
          wait_vm<WINV - 1>();
          if (((n - WINV + 1) & 1) == 0) prog_set(&L.vprog[w], (n - WINV + 1) >> 1, lane);
        }
        dma16(sv, voff, kvbase + (n >> 1) * 8192 + w * 2048 + (n & 1) * 1024, lb + n * 1024);
      }
      lds_signal(&L.ctrl[C_VI], lane);
#pragma unroll
      for (int n = 32; n < 32 + WINV; n++) {
        if (((n - WINV + 1) & 1) == 0) {
          asm volatile("s_waitcnt vmcnt(%0)" ::"n"(31 - (n - WINV)) : "memory");
          prog_set(&L.vprog[w], (n - WINV + 1) >> 1, lane);
        }
      }
    }
    e = 1;
  }

  {
    ARGS_RELOAD();
    const u32x4 T = read_tag(L.tag);
    const rsrc_t rws = mk_rsrc(P_WS, WS_END);
    const int pool = (opaque(blockIdx.x) >> 3) & (NPOOL - 1);
    const u32 vo = (u32)(g * 2048 + ((((lane & 15) >> 1) ^ (4 * (a & 1) + g)) * 32) + (lane & 1) * 16);
    lds_wait<2>(&L.ctrl[C_U + e], 1);
    const u32 tk = __builtin_amdgcn_readfirstlane(L.ctrl[C_U + e]) - 1;
    if (tk >= NU) { lds_post(&L.ctrl[C_ED]); return; }
    const bool isO = tk >= ND;
    const u32 jr = unit_row(pool, tk), cq = unit_col(pool, tk);
    u32 fpo = WS_FP + (cq * (isO ? 64 : 32) * 64 + (isO ? 0 : 32) + jr) * 128 + (8 * a + (hi ? 4 : 0) + g) * 8192 + (lane & 7) * 16;
    constexpr int NH = 2;
    const u32 ho = WS_H + (jr * (4 * NH) + a) * 1024u + lane * 16;
    u32x4 p[NH];
    {
      const u32x4 sw = mk_srd(P_WS, WS_END);
      const u32 h0 = isO ? 0x80000000u : ho;
#pragma unroll
      for (int i = 0; i < NH; i++)
        asm volatile("buffer_load_dwordx4 %0, %1, %2, %3 offen " LSTR : "=&v"(p[i]) : "v"(h0), "s"(sw), "s"(i * 4096) : "memory");
    }
    {
      lds_wait<2>(&L.ctrl[C_VI], 4);
      prog_wait(L.pvprog, 8 + 8 * e);
      const u32x4 sd = {isO ? ka[14] : kb[0], (isO ? ka[15] : kb[1]) & 0xFFFFu, isO ? WQ_BYTES : WG_BYTES, 0x00020000u};
      const bool kd = !isO;
      const u32 tbase = (jr * (kd ? 256 : 128) + a * 32) * 2048u + cq * (kd ? 256 : 512);
      const u32 tb1 = tbase + (kd ? 262144u : 256u);
      const u32 lb = img_addr + a * 32768 + e * 16384;
#pragma unroll
      for (int n = 0; n < 16; n++) {
        if (n >= WINE) wait_vm<WINE - 1>();
        if (n == TK2 && wave == 8) prog_set(&L.ctrl[C_EI], 1, lane);
        const int rg = (n >> 2) + 4 * ((n >> 1) & 1);
        dma16(sd, vo, ((n & 1) ? tb1 : tbase) + rg * 8192, lb + (n & 1) * 8192 + rg * 1024);
      }
    }
    static_assert(WINE <= 8, "window");
#pragma unroll
    for (int i = 0; i < NH; i++) asm volatile("" : "+v"(p[i]));
    if (!isO) {
      bool need[NH], any = false;
#pragma unroll
      for (int i = 0; i < NH; i++) { need[i] = (tdiff(p[i], T) & TMASK) != 0; any |= need[i]; }
      if (__any(any)) {
#pragma nounroll
        for (int it = 0; it < GLB_SPIN_CAP; it++) {
          CBAR();
#pragma unroll
          for (int i = 0; i < NH; i++)
            if (need[i]) p[i] = ld_sc1(rws, ho, i * 4096);
          any = false;
#pragma unroll
          for (int i = 0; i < NH; i++) { need[i] = (tdiff(p[i], T) & TMASK) != 0; any |= need[i]; }
          if (!__any(any)) break;
          __builtin_amdgcn_s_sleep(PSLEEP);
        }
      }
      CBAR();
#pragma unroll
      for (int i = 0; i < NH; i++)
        ((u32x2*)&L.bfr[e][i * 128 + (g * 4 + a) * 8 + (lane & 7)])[hi ? 1 : 0] =
            u32x2{__builtin_amdgcn_perm(p[i][1], p[i][0], 0x07060302u), __builtin_amdgcn_perm(p[i][3], p[i][2], 0x07060302u)};
      lds_post(&L.ctrl[C_OF + e]);
    }
    u32x4 F[4][4];
    const int q = (lane >> 2) & 3;
    const lds_ptr b0 = img + g * 32768 + e * 16384 + q * 256 + (((2 * a) ^ (4 * (g & 1) + q)) * 32) + (lane & 3) * 8;
    const lds_ptr b1 = img + g * 32768 + e * 16384 + q * 256 + (((2 * a + 1) ^ (4 * (g & 1) + q)) * 32) + (lane & 3) * 8;
    wait_vm<8>();
    prog_set(&L.eprog[e][a], 2, lane);
    prog_wait(L.eprog[e], 2);
    tile_frags(b0, b1, 0, F[0]);
    tile_frags(b0, b1, 1, F[1]);
    u32 br = lds_addr(&L.bfr[e][g * 8 + (lane & 7)]);
    u32x4 Tv = T & TMASK;
    if (!isO) {
      u32x4 bf[8];
#pragma nounroll
      for (int it = 0; it < LDS_SPIN_CAP; it++) {
        u32 fl;
        asm volatile("ds_read_b32 %0, %9\n\tds_read_b128 %1, %10\n\tds_read_b128 %2, %10 offset:512\n\tds_read_b128 %3, %10 offset:1024\n\t"
                     "ds_read_b128 %4, %10 offset:1536\n\tds_read_b128 %5, %10 offset:2048\n\tds_read_b128 %6, %10 offset:2560\n\t"
                     "ds_read_b128 %7, %10 offset:3072\n\tds_read_b128 %8, %10 offset:3584\n\ts_waitcnt lgkmcnt(0)"
                     : "=&v"(fl), "=&v"(bf[0]), "=&v"(bf[1]), "=&v"(bf[2]), "=&v"(bf[3]), "=&v"(bf[4]), "=&v"(bf[5]), "=&v"(bf[6]), "=&v"(bf[7])
                     : "v"(lds_addr(&L.ctrl[C_OF + e])), "v"(br) : "memory");
        if (__builtin_amdgcn_readfirstlane(fl) >= 4) break;
        __builtin_amdgcn_s_sleep(1);
      }
      f32x4 c0 = zero4, c1 = zero4, c2 = zero4, c3 = zero4;
#pragma unroll
      for (int kq = 0; kq < 2; kq++) {
        c0 = mfma16(F[kq][0], bf[kq], c0);
        c1 = mfma16(F[kq][1], bf[kq], c1);
        c0 = mfma16(F[kq][2], bf[4 + kq], c0);
        c1 = mfma16(F[kq][3], bf[4 + kq], c1);
      }
      pin(fpo); pin(Tv); pin(c0); pin(c1);
      wait_vm<0>();
      prog_set(&L.eprog[e][a], 4, lane);
      __builtin_amdgcn_s_setprio(2);
      prog_wait<0>(L.eprog[e], 4);
      tile_frags(b0, b1, 2, F[2]);
      tile_frags(b0, b1, 3, F[3]);
#pragma unroll
      for (int kq = 2; kq < 4; kq++) {
        c0 = mfma16(F[kq][0], bf[kq], c0);
        c1 = mfma16(F[kq][1], bf[kq], c1);
        c2 = mfma16(F[kq][2], bf[4 + kq], c2);
        c3 = mfma16(F[kq][3], bf[4 + kq], c3);
      }
      u32x4 r0;
#pragma unroll
      for (int i = 0; i < 4; i++) r0[i] = __builtin_amdgcn_bitop3_b32(f2u((hi ? c1[i] : c0[i]) + (hi ? c3[i] : c2[i])), TMASK, Tv[i], 0xBA);
      st_sc1(rws, fpo, r0);
      lds_post(&L.ctrl[C_ED]);
      return;
    }
    const int ob = 2 * a + (g & 1);
    u32 oo = WS_OP + ((jr >> 3) * 8 + ob) * 16384 + (jr & 7) * 2048 + (lane >> 5) * 1024 + (lane & 15) * 16;
    u32 so = WS_ST + (((jr >> 3) * 8 + ob) * 8 + ((lane & 7) ^ (4 * (lane >> 5)))) * 128 + (jr & 7) * 16;
    u32 bw = lds_addr(&L.bfr[e][((lane & 3) * 4 + ((lane >> 2) & 3)) * 8 + ob]) + (lane >> 5) * 8;
    lds_wait(&L.ctrl[C_QD], 1);
    pin(oo); pin(so); pin(bw); pin(br); pin(fpo); pin(Tv);
    wait_vm<0>();
    u32x4 od[4], sd;
    sd = ld_sc1(rws, so, 0);
#pragma unroll
    for (int i = 0; i < 4; i++) od[i] = ld_sc1(rws, oo + i * 256, 0);
    prog_set(&L.eprog[e][a], 4, lane);
    __builtin_amdgcn_s_setprio(2);
    prog_wait<0>(L.eprog[e], 4);
    tile_frags(b0, b1, 2, F[2]);
    tile_frags(b0, b1, 3, F[3]);
    if (isO) {
      bool need[5];
#pragma unroll
      for (int i = 0; i < 4; i++) need[i] = (tdiff(od[i], T) & TMASK) == 0;
      need[4] = ((sd[2] ^ T[2]) | (sd[3] ^ T[3])) != 0;
      if (__any(need[0] | need[1] | need[2] | need[3] | need[4])) {
#pragma nounroll
        for (int it = 0; it < GLB_SPIN_CAP; it++) {
          CBAR();
          if (need[4]) sd = ld_sc1(rws, so, 0);
#pragma unroll
          for (int i = 0; i < 4; i++)
            if (need[i]) od[i] = ld_sc1(rws, oo + i * 256, 0);
          __builtin_amdgcn_sched_barrier(0);
#pragma unroll
          for (int i = 0; i < 4; i++) need[i] = (tdiff(od[i], T) & TMASK) == 0;
          need[4] = ((sd[2] ^ T[2]) | (sd[3] ^ T[3])) != 0;
          if (!__any(need[0] | need[1] | need[2] | need[3] | need[4])) break;
          __builtin_amdgcn_s_sleep(OSL);
        }
        CBAR();
      }
      const float mm = u2f(sd[0]);
      float M = fmaxf(mm, dpp<XOR1>(mm));
      M = fmaxf(M, dpp<XOR2>(M));
      M = fmaxf(M, dpp<XOR7>(M));
      const float ew = __expf(mm - M);
      float Ls = ew * u2f(sd[1]);
      Ls += dpp<XOR1>(Ls);
      Ls += dpp<XOR2>(Ls);
      Ls += dpp<XOR7>(Ls);
      const float wv = ew / Ls;
      const f32x4 wq4 = {dpp<0x150>(wv), dpp<0x151>(wv), dpp<0x152>(wv), dpp<0x153>(wv)};
      f32x2 ov[4];
#pragma unroll
      for (int i = 0; i < 4; i++) {
        ov[i] = f32x2{bflo(od[0][i]), bfhi(od[0][i])} * wq4[0];
#pragma unroll
        for (int k = 1; k < 4; k++) ov[i] = __builtin_elementwise_fma(f32x2{bflo(od[k][i]), bfhi(od[k][i])}, f32x2{wq4[k], wq4[k]}, ov[i]);
      }
      u32x2 ow;
#pragma unroll
      for (int i = 0; i < 2; i++) {
        const auto t0 = __builtin_amdgcn_permlane32_swap(f2u(ov[i][0]), f2u(ov[i + 2][0]), false, false);
        const auto t1 = __builtin_amdgcn_permlane32_swap(f2u(ov[i][1]), f2u(ov[i + 2][1]), false, false);
        const f32x2 z = f32x2{u2f(t0[0]), u2f(t1[0])} + f32x2{u2f(t0[1]), u2f(t1[1])};
        ow[i] = pack2(z[0], z[1]);
      }
      asm volatile("ds_write_b64 %0, %1" ::"v"(bw), "v"(ow) : "memory");
      lds_post(&L.ctrl[C_OF + e]);
    }
    u32x4 bf[4];
#pragma nounroll
    for (int it = 0; it < LDS_SPIN_CAP; it++) {
      u32 fl;
      asm volatile("ds_read_b32 %0, %5\n\tds_read_b128 %1, %6\n\tds_read_b128 %2, %6 offset:512\n\tds_read_b128 %3, %6 offset:1024\n\t"
                   "ds_read_b128 %4, %6 offset:1536\n\ts_waitcnt lgkmcnt(0)"
                   : "=&v"(fl), "=&v"(bf[0]), "=&v"(bf[1]), "=&v"(bf[2]), "=&v"(bf[3]) : "v"(lds_addr(&L.ctrl[C_OF + e])), "v"(br) : "memory");
      if (__builtin_amdgcn_readfirstlane(fl) >= 4) break;
    }
    f32x4 c0 = zero4, c1 = zero4, c2 = zero4, c3 = zero4;
#pragma unroll
    for (int kq = 0; kq < 4; kq++) {
      c0 = mfma16(F[kq][0], bf[kq], c0);
      c1 = mfma16(F[kq][1], bf[kq], c1);
      c2 = mfma16(F[kq][2], bf[kq], c2);
      c3 = mfma16(F[kq][3], bf[kq], c3);
    }
    u32x4 r0, r1;
#pragma unroll
    for (int i = 0; i < 4; i++) {
      r0[i] = __builtin_amdgcn_bitop3_b32(f2u(hi ? c1[i] : c0[i]), TMASK, Tv[i], 0xBA);
      r1[i] = __builtin_amdgcn_bitop3_b32(f2u(hi ? c3[i] : c2[i]), TMASK, Tv[i], 0xBA);
    }
    st_sc1(rws, fpo, r0);
    __builtin_amdgcn_raw_buffer_store_b128(r1, rws, (int)fpo, 32 * 8192, AUX_SC1);
    if (isO && a == 0) lds_post(&L.ctrl[C_F1]);
    lds_post(&L.ctrl[C_ED]);
  }
}

extern "C" void kernel_launch(void* const* d_in, const int* in_sizes, int n_in,
                              void* d_out, int out_size, void* d_ws, size_t ws_size,
                              hipStream_t stream) {
  fused_decode_kernel<<<256, NTHR, 0, stream>>>((const u16*)d_in[0], (const u16*)d_in[3], (const u16*)d_in[6],
                                               (const u16*)d_in[7], (unsigned char*)d_ws, (const u16*)d_in[1],
                                               (const u16*)d_in[2], (const u16*)d_in[4], (const u16*)d_in[8],
                                               (u16*)d_out);
}
```
